```python
import torch

def _softmax(x, axis=-1):
    return torch.softmax(x, dim=axis)
def _gelu(x):
    return torch.nn.functional.gelu(x, approximate="tanh")
def _silu(x):
    return torch.nn.functional.silu(x)
def _sigmoid(x):
    return torch.sigmoid(x)
def _layernorm(x, axis=-1, eps=1e-5):
    m = x.mean(dim=axis, keepdim=True)
    v = x.var(dim=axis, keepdim=True, unbiased=False)
    return (x - m) / torch.sqrt(v + eps)
def _scatter_add(tgt, idx, src, axis=0):
    return tgt.clone().index_add_(axis, idx, src)
def _segment_sum(src, seg, n):
    out = torch.zeros((n,) + src.shape[1:], dtype=src.dtype)
    return out.index_add_(0, seg, src)
def _pad(x, pw):
    flat = tuple(v for pair in reversed(pw) for v in pair)
    return torch.nn.functional.pad(x, flat)

kernel_name = 'gated_mix__ssm_conv_s4d__ssm_conv_h3__prompt'

def setup_inputs():

    _w = (lambda a: (lambda t: t.to(torch.bfloat16) if t.is_floating_point() else t)(torch.as_tensor(a)))
    def _A_setup():
        Bsz, L, D, N = 1, 2048, 1024, 128
        return {
            "u": _w((lambda *s: torch.randn(*s, dtype=torch.bfloat16) * 0.05)(Bsz, L, D)),
            "log_A_real": _w((lambda *s: torch.randn(*s, dtype=torch.bfloat16) * 3.2)(D, N) * 0.5),
            "A_imag": _w((lambda *s: torch.randn(*s, dtype=torch.bfloat16) * 0.05)(D, N) * 3.14),
            "C_real": _w((lambda *s: torch.randn(*s, dtype=torch.bfloat16) * 0.1)(D, N) / N ** 0.5),
            "C_imag": _w((lambda *s: torch.randn(*s, dtype=torch.bfloat16) * 0.1)(D, N) / N ** 0.5),
            "log_dt": _w((lambda *s: torch.randn(*s, dtype=torch.bfloat16) * 0.05)(D) * 0.5 - 4.0),
            "D_skip": _w((lambda *s: torch.randn(*s, dtype=torch.bfloat16) * 0.05)(D)),
        }
    _A_in = _A_setup()

    def _B_setup():
        B, S, D = 1, 2048, 1024
        ks = 4
        return {
            "q": _w((lambda *s: torch.randn(*s, dtype=torch.bfloat16) * 0.05)(B, S, D)),
            "k": _w((lambda *s: torch.randn(*s, dtype=torch.bfloat16) * 0.4)(B, S, D)),
            "v": _w((lambda *s: torch.randn(*s, dtype=torch.bfloat16) * 0.2)(B, S, D)),
            "shift_k": _w((lambda *s: torch.randn(*s, dtype=torch.bfloat16) * 0.4)(D, ks) * 0.5),
            "ssm_k": _w((lambda *s: torch.randn(*s, dtype=torch.bfloat16) * 0.4)(D, S) * (1.0 / S ** 0.5)),
            "ssm_D": _w((lambda *s: torch.randn(*s, dtype=torch.bfloat16) * 0.4)(D)),
        }
    _B_in = _B_setup()

    return {
        "A_u": _A_in["u"],
        "A_log_A_real": _A_in["log_A_real"],
        "A_A_imag": _A_in["A_imag"],
        "A_C_real": _A_in["C_real"],
        "A_C_imag": _A_in["C_imag"],
        "A_log_dt": _A_in["log_dt"],
        "A_D_skip": _A_in["D_skip"],
        "B_q": _B_in["q"],
        "B_k": _B_in["k"],
        "B_v": _B_in["v"],
        "B_shift_k": _B_in["shift_k"],
        "B_ssm_k": _B_in["ssm_k"],
        "B_ssm_D": _B_in["ssm_D"],
        "gate_w": _w((lambda *s: torch.randn(*s, dtype=torch.bfloat16) * 0.05)(1024) * 16.0),
    }

def reference(A_u, A_log_A_real, A_A_imag, A_C_real, A_C_imag, A_log_dt, A_D_skip, B_q, B_k, B_v, B_shift_k, B_ssm_k, B_ssm_D, gate_w):
    def _A_ref(u, log_A_real, A_imag, C_real, C_imag, log_dt, D_skip):
        L = u.shape[1]
        dt = torch.exp(log_dt)
        A = -torch.exp(log_A_real) + 1j * A_imag
        C = (C_real + 1j * C_imag) * dt[:, None]
        pos = (lambda x, dt: x.to(dt))(torch.arange(L), log_dt.dtype)
        P = torch.exp((A * dt[:, None])[:, :, None] * pos[None, None, :])
        K = 2.0 * torch.einsum("dn,dnl->dl", C, P).real
        L2 = 2 * L
        u_t = (lambda x, *d: x.permute(*d))(u, 0, 2, 1)
        Kf = (lambda x, *a, **k: torch.fft.rfft(x.float(), *a, **k))(K, L2)
        Uf = (lambda x, *a, **k: torch.fft.rfft(x.float(), *a, **k))(u_t, L2)
        y = (lambda x, *a, **k: torch.fft.irfft(x, *a, **k).to(torch.bfloat16))(Uf * Kf[None, :, :], L2)[:, :, :L]
        y = (lambda x, *d: x.permute(*d))(y, 0, 2, 1)
        return y + u * D_skip[None, None, :]

    def _B_ref(q, k, v, shift_k, ssm_k, ssm_D):
        B, S, D = q.shape
        ks = shift_k.shape[-1]
        k_pad = _pad(k, ((0, 0), (ks - 1, 0), (0, 0)))
        k_shift = torch.zeros_like(k)
        for t in range(ks):
            k_shift = k_shift + k_pad[:, t:t + S, :] * shift_k[:, ks - 1 - t][None, None, :]
        kv = k_shift * v
        L = 2 * S
        kv_pad = _pad(kv, ((0, 0), (0, S), (0, 0)))
        kf_pad = _pad(ssm_k, ((0, 0), (0, S)))
        kv_f = (lambda x, axes=(-2,-1): torch.fft.rfft2(x.float(), dim=axes))(kv_pad[:, None, :, :], axes=(1, 2))
        kf_f = (lambda x, axes=(-2,-1): torch.fft.rfft2(x.float(), dim=axes))(kf_pad[:, None, :], axes=(1, 2))
        kf_f = (lambda x, *d: x.permute(*d))(kf_f, 1, 2, 0)
        y_f = kv_f * kf_f[None, :, :, :]
        y = (lambda x, s, axes=(-2,-1): torch.fft.irfft2(x, s=s, dim=axes).to(torch.bfloat16))(y_f, (1, L), axes=(1, 2))[:, 0, :S, :]
        y = y + kv * ssm_D[None, None, :]
        return q * y

    x = A_u
    g = _sigmoid((x * gate_w).sum(-1, keepdims=True))
    a = _A_ref(x, A_log_A_real, A_A_imag, A_C_real, A_C_imag, A_log_dt, A_D_skip)
    b = _B_ref(x, B_k, B_v, B_shift_k, B_ssm_k, B_ssm_D)
    return g * a + (1 - g) * b
```

```cpp
#include <hip/hip_runtime.h>
#include <hip/hip_bf16.h>


typedef unsigned int u32;
typedef unsigned short u16;
typedef unsigned long long u64;
typedef u32 u32x2 __attribute__((ext_vector_type(2)));
typedef u32 u32x4 __attribute__((ext_vector_type(4)));
typedef float f32x2 __attribute__((ext_vector_type(2)));
typedef float f32x4 __attribute__((ext_vector_type(4)));
typedef __bf16 bf16x2 __attribute__((ext_vector_type(2)));
typedef __bf16 bf16x8 __attribute__((ext_vector_type(8)));
typedef short s16x4 __attribute__((ext_vector_type(4)));
#define DI __device__ __forceinline__
#define LDS __attribute__((address_space(3)))

constexpr int L = 2048, D = 1024, NM = 128;
constexpr int KB_STRIDE = 4128;
constexpr int XP_STRIDE = 1184;
constexpr int XB_STRIDE = 4 * XP_STRIDE;
constexpr int TB_ROW = 528;
constexpr int TB_STRIDE = 16 * TB_ROW;
constexpr int OFF_K = 0;
constexpr int OFF_X = OFF_K + 8 * KB_STRIDE;
constexpr int OFF_T = OFF_X + 8 * XB_STRIDE;
constexpr int LDS_BYTES = OFF_T + 4 * TB_STRIDE;

DI u32 cvtpk(float lo, float hi) { f32x2 v = {lo, hi}; bf16x2 b = __builtin_convertvector(v, bf16x2); return __builtin_bit_cast(u32, b); }
DI float blo(u32 x) { return __uint_as_float(x << 16); }
DI float bhi(u32 x) { return __uint_as_float(x & 0xffff0000u); }
DI float rb(float x) { return blo(cvtpk(x, x)); }
DI float bf(u16 x) { return __uint_as_float((u32)x << 16); }
DI f32x4 mfma(u32x4 a, u32x4 b, f32x4 c) {
  return __builtin_amdgcn_mfma_f32_16x16x32_bf16(__builtin_bit_cast(bf16x8, a), __builtin_bit_cast(bf16x8, b), c, 0, 0, 0);
}
typedef LDS char* lptr;
DI u32x2 lds_r64(lptr p) { return *(LDS u32x2*)p; }
DI u32x4 lds_r128(lptr p) { return *(LDS u32x4*)p; }
DI void lds_w32(lptr p, u32 v) { *(LDS u32*)p = v; }
DI void lds_w16(lptr p, u16 v) { *(LDS u16*)p = v; }
DI void lds_w64(lptr p, u32x2 v) { *(LDS u32x2*)p = v; }
DI void lds_w128(lptr p, u32x4 v) { *(LDS u32x4*)p = v; }
DI u32x2 lds_tr(lptr p) {
  typedef short v4i16_t __attribute__((ext_vector_type(4)));
  return __builtin_bit_cast(u32x2, __builtin_amdgcn_ds_read_tr16_b64_v4i16((LDS v4i16_t*)p));
}
DI void wsync() { __builtin_amdgcn_fence(__ATOMIC_ACQ_REL, "wavefront"); __builtin_amdgcn_wave_barrier(); }
DI u32 perm(u32 hi, u32 lo, u32 sel) { return __builtin_amdgcn_perm(hi, lo, sel); }

struct cplx { f32x2 r, i; };
DI cplx cmul(cplx a, cplx b) { cplx c; c.r = a.r * b.r - a.i * b.i; c.i = a.r * b.i + a.i * b.r; return c; }
DI cplx csq(cplx a) { cplx c; c.r = a.r * a.r - a.i * a.i; f32x2 t = a.r * a.i; c.i = t + t; return c; }
DI cplx conj(cplx a) { cplx c; c.r = a.r; c.i = -a.i; return c; }
DI cplx cexp_p(f32x2 zr2, f32x2 zi_rev, float p) {
  cplx c;
#pragma unroll
  for (int m = 0; m < 2; m++) {
    float mag = __builtin_amdgcn_exp2f(zr2[m] * p);
    float ph = __builtin_amdgcn_fractf(zi_rev[m] * p);
    c.r[m] = mag * __builtin_amdgcn_cosf(ph);
    c.i[m] = mag * __builtin_amdgcn_sinf(ph);
  }
  return c;
}
DI void tput(lptr trow, cplx e) { u32x2 v; v[0] = cvtpk(e.r[0], e.i[0]); v[1] = cvtpk(e.r[1], e.i[1]); lds_w64(trow, v); }

__global__ __launch_bounds__(512) void gate_kernel(const u32x4* __restrict__ u, const u32x4* __restrict__ w, float* __restrict__ g) {
  int row = blockIdx.x * 8 + (threadIdx.x >> 6), lane = threadIdx.x & 63;
  float s = 0.f;
#pragma unroll
  for (int h = 0; h < 2; h++) {
    u32x4 x = u[row * 128 + h * 64 + lane], ww = w[h * 64 + lane];
#pragma unroll
    for (int e = 0; e < 4; e++) { u32 p = cvtpk(blo(x[e]) * blo(ww[e]), bhi(x[e]) * bhi(ww[e])); s += blo(p) + bhi(p); }
  }
#pragma unroll
  for (int o = 32; o >= 1; o >>= 1) s += __shfl_xor(s, o);
  if (lane == 0) { float z = rb(s); g[row] = rb(1.f / (1.f + __expf(-z))); }
}

__global__ __launch_bounds__(512, 2) void conv_kernel(
    const u32x2* __restrict__ gu, const u16* __restrict__ logAr, const u16* __restrict__ Aim, const u16* __restrict__ Cre,
    const u16* __restrict__ Cim, const u16* __restrict__ logdt, const u16* __restrict__ Dskip, const u32x2* __restrict__ gk,
    const u32x2* __restrict__ gv, const u16* __restrict__ shk, const u32x4* __restrict__ ssmk, const u16* __restrict__ ssmD,
    const float* __restrict__ gate, u32x2* __restrict__ out) {
  extern __shared__ __attribute__((aligned(16))) char smem_[];
  lptr smem = (lptr)smem_;
  const int b = blockIdx.x, cg = (b & 7) * 32 + (b >> 3);
  const int tid = threadIdx.x, lane = tid & 63, w = __builtin_amdgcn_readfirstlane(tid >> 6);
  const int ch = cg * 4 + (w & 3);
  const int j16 = lane & 15, q = lane >> 4;
  lptr kb = smem + OFF_K + w * KB_STRIDE;

  u32 pAr = 0, pAi = 0, pCr = 0, pCi = 0; u16 pdt = 0;
  u32x4 kk[4];
  if (w < 4) {
    pAr = ((const u32*)logAr)[ch * 64 + lane]; pAi = ((const u32*)Aim)[ch * 64 + lane];
    pCr = ((const u32*)Cre)[ch * 64 + lane]; pCi = ((const u32*)Cim)[ch * 64 + lane];
    pdt = logdt[ch];
  } else {
#pragma unroll
    for (int e = 0; e < 4; e++) kk[e] = ssmk[ch * 256 + e * 64 + lane];
  }
  u32x2 xu[4], xk[4], xv[4], hk[3];
  const int r0 = tid * 4;
#pragma unroll
  for (int e = 0; e < 4; e++) xu[e] = gu[(r0 + e) * 256 + cg];
#pragma unroll
  for (int e = 0; e < 4; e++) xk[e] = gk[(r0 + e) * 256 + cg];
#pragma unroll
  for (int e = 0; e < 4; e++) xv[e] = gv[(r0 + e) * 256 + cg];
#pragma unroll
  for (int e = 0; e < 3; e++) { hk[e] = u32x2{0, 0}; if (lane == 0 && tid > 0) hk[e] = gk[(r0 - 3 + e) * 256 + cg]; }
  u32x4 shw0 = ((const u32x4*)shk)[cg * 2], shw1 = ((const u32x4*)shk)[cg * 2 + 1];
  u32x2 dsk = ((const u32x2*)Dskip)[cg], dss = ((const u32x2*)ssmD)[cg];

  if (tid < 64) lds_w32(smem + OFF_K + (tid >> 3) * KB_STRIDE + (tid & 7) * 4, 0);
  for (int i = tid; i < 32 * 32; i += 512) lds_w32(smem + OFF_X + (i >> 5) * XP_STRIDE + (i & 31) * 4, 0);

  if (w < 4) {
    float dt = rb(expf(bf(pdt)));
    f32x2 zr, zi; cplx C;
    zr[0] = -rb(expf(blo(pAr))) * dt; zr[1] = -rb(expf(bhi(pAr))) * dt;
    zi[0] = blo(pAi) * dt; zi[1] = bhi(pAi) * dt;
    C.r[0] = 2.f * blo(pCr) * dt; C.r[1] = 2.f * bhi(pCr) * dt;
    C.i[0] = -2.f * blo(pCi) * dt; C.i[1] = -2.f * bhi(pCi) * dt;
    zr *= 1.44269504088896341f; zi *= 0.15915494309189535f;
    lptr tb = smem + OFF_T + w * TB_STRIDE;
    lptr tw = tb + lane * 8, tr = tb + j16 * TB_ROW + q * 16;
    cplx w1 = cexp_p(zr, zi, 1.f), w16 = cexp_p(zr, zi, 16.f), w256 = cexp_p(zr, zi, 256.f);
    u32x4 G[8];
    {
      cplx g; g.r = f32x2{1.f, 1.f}; g.i = f32x2{0.f, 0.f};
      tput(tw, g); g = w16; tput(tw + TB_ROW, g);
#pragma unroll
      for (int i = 2; i < 16; i++) { g = cmul(g, w16); tput(tw + i * TB_ROW, g); }
      wsync();
#pragma unroll
      for (int s = 0; s < 8; s++) G[s] = lds_r128(tr + s * 64);
      wsync();
    }
    w1 = conj(w1); w256 = conj(w256);
    cplx w2 = csq(w1), w4 = csq(w2), w8 = csq(w4), w512 = csq(w256), w1024 = csq(w512);
    lptr kd = kb + 32;
    {
      cplx e = C; tput(tw, e);
#pragma unroll
      for (int i = 1; i < 16; i++) { e = cmul(e, w1); tput(tw + i * TB_ROW, e); }
      f32x4 a = {0, 0, 0, 0};
      wsync();
#pragma unroll
      for (int s = 0; s < 8; s++) a = mfma(lds_r128(tr + s * 64), G[s], a);
      wsync();
      lds_w64(kd + (16 * j16 + 4 * q) * 2, u32x2{cvtpk(a[0], a[1]), cvtpk(a[2], a[3])});
    }
    {
      cplx e = cmul(C, w256); tput(tw, e);
#pragma unroll
      for (int i = 1; i < 8; i++) { e = cmul(e, w2); tput(tw + i * TB_ROW, e); }
      e = cmul(C, w512);
#pragma unroll
      for (int i = 0; i < 4; i++) { tput(tw + (8 + i) * TB_ROW, e); tput(tw + (12 + i) * TB_ROW, cmul(e, w256)); if (i < 3) e = cmul(e, w4); }
      f32x4 a = {0, 0, 0, 0};
      wsync();
#pragma unroll
      for (int s = 0; s < 8; s++) a = mfma(lds_r128(tr + s * 64), G[s], a);
      wsync();
      u32 p01 = cvtpk(a[0], a[1]), p23 = cvtpk(a[2], a[3]);
      u32 v00 = perm(p01, p01, 0x01000100), v11 = perm(p01, p01, 0x03020302), v22 = perm(p23, p23, 0x01000100), v33 = perm(p23, p23, 0x03020302);
      const int base = q < 2 ? 256 + 16 * j16 + 8 * q : 512 + 256 * (q - 2) + 16 * j16;
      lds_w128(kd + base * 2, q < 2 ? u32x4{v00, perm(p23, p01, 0x05040302), v22, v33} : u32x4{v00, p01, v11, v22});
      if (q >= 2) lds_w128(kd + base * 2 + 16, u32x4{v22, p23, v33, v33});
      wsync();
      if (q >= 2 && base > 512) lds_w32(kd + (base - 2) * 2, v00); else if (base > 256) lds_w16(kd + (base - 1) * 2, (u16)p01);
      wsync();
    }
    {
      cplx e0 = cmul(C, w1024), e1 = cmul(e0, w8);
#pragma unroll
      for (int i = 0; i < 4; i++) { tput(tw + (2 * i) * TB_ROW, e0); tput(tw + (2 * i + 1) * TB_ROW, e1); e0 = cmul(e0, w256); if (i < 3) e1 = cmul(e1, w256); }
      tput(tw + 8 * TB_ROW, e0);
      f32x4 a = {0, 0, 0, 0};
      wsync();
#pragma unroll
      for (int s = 0; s < 8; s++) a = mfma(lds_r128(tr + s * 64), G[s], a);
      wsync();
      u32 p01 = cvtpk(a[0], a[1]), p23 = cvtpk(a[2], a[3]);
      if (q < 2) {
#pragma unroll
        for (int hh = 0; hh < 2; hh++) {
          u32 p = hh ? p23 : p01;
          u32 v00 = perm(p, p, 0x01000100), v11 = perm(p, p, 0x03020302);
          int base = 1024 + 512 * q + 256 * hh + 16 * j16;
          lds_w128(kd + base * 2, u32x4{v00, v00, p, v11});
          lds_w128(kd + base * 2 + 16, u32x4{v11, v11, v11, v11});
        }
      }
      wsync();
      if (q < 2) {
#pragma unroll
        for (int hh = 0; hh < 2; hh++) {
          u32 p = hh ? p23 : p01;
          u32 v00 = perm(p, p, 0x01000100);
          int base = 1024 + 512 * q + 256 * hh + 16 * j16;
          if (base > 1024) lds_w64(kd + (base - 4) * 2, u32x2{v00, v00}); else lds_w32(kd + (base - 2) * 2, v00);
        }
      } else if (lane == 32) {
        u32 v00 = perm(p01, p01, 0x01000100);
        lds_w64(kd + 2044 * 2, u32x2{v00, v00});
      }
    }
  } else {
#pragma unroll
    for (int e = 0; e < 4; e++) lds_w128(kb + 32 + (e * 64 + lane) * 16, kk[e]);
  }

  u32x2 xkv[4];
  {
    u32x2 kr[7];
#pragma unroll
    for (int e = 0; e < 3; e++) {
      kr[e][0] = __builtin_amdgcn_update_dpp(hk[e][0], xk[e + 1][0], 0x138, 0xf, 0xf, false);
      kr[e][1] = __builtin_amdgcn_update_dpp(hk[e][1], xk[e + 1][1], 0x138, 0xf, 0xf, false);
    }
#pragma unroll
    for (int e = 0; e < 4; e++) kr[3 + e] = xk[e];
#pragma unroll
    for (int cp = 0; cp < 2; cp++) {
      u32x4 sw = cp ? shw1 : shw0;
      f32x2 t0 = {blo(sw[0]), blo(sw[2])}, t1 = {bhi(sw[0]), bhi(sw[2])}, t2 = {blo(sw[1]), blo(sw[3])}, t3 = {bhi(sw[1]), bhi(sw[3])};
      f32x2 kf[7];
#pragma unroll
      for (int e = 0; e < 7; e++) kf[e] = f32x2{blo(kr[e][cp]), bhi(kr[e][cp])};
#pragma unroll
      for (int e = 0; e < 4; e++) {
        f32x2 p3 = kf[e] * t3, p2 = kf[e + 1] * t2, p1 = kf[e + 2] * t1, p0 = kf[e + 3] * t0;
        u32 u3 = cvtpk(p3[0], p3[1]), u2 = cvtpk(p2[0], p2[1]), u1 = cvtpk(p1[0], p1[1]), u0 = cvtpk(p0[0], p0[1]);
        f32x2 s = f32x2{blo(u3), bhi(u3)} + f32x2{blo(u2), bhi(u2)};
        u32 us = cvtpk(s[0], s[1]);
        s = f32x2{blo(us), bhi(us)} + f32x2{blo(u1), bhi(u1)}; us = cvtpk(s[0], s[1]);
        s = f32x2{blo(us), bhi(us)} + f32x2{blo(u0), bhi(u0)}; us = cvtpk(s[0], s[1]);
        s = f32x2{blo(us), bhi(us)} * f32x2{blo(xv[e][cp]), bhi(xv[e][cp])};
        xkv[e][cp] = cvtpk(s[0], s[1]);
      }
    }
  }
  {
    const bool odd = lane & 1;
    lptr xw = smem + OFF_X + (odd ? 2 * XP_STRIDE : 0) + 128 + (tid >> 1) * 4;
#pragma unroll
    for (int arr = 0; arr < 2; arr++) {
#pragma unroll
      for (int cp = 0; cp < 2; cp++) {
#pragma unroll
        for (int a = 0; a < 2; a++) {
          u32 own_keep = arr ? (odd ? xkv[2 + a][cp] : xkv[a][cp]) : (odd ? xu[2 + a][cp] : xu[a][cp]);
          u32 own_send = arr ? (odd ? xkv[a][cp] : xkv[2 + a][cp]) : (odd ? xu[a][cp] : xu[2 + a][cp]);
          u32 other = __builtin_amdgcn_update_dpp(0u, own_send, 0xb1, 0xf, 0xf, true);
          u32 ev = odd ? other : own_keep, od = odd ? own_keep : other;
          lds_w32(xw + (arr * 4 + cp * 2) * XB_STRIDE + a * XP_STRIDE, perm(od, ev, 0x05040100));
          lds_w32(xw + (arr * 4 + cp * 2 + 1) * XB_STRIDE + a * XP_STRIDE, perm(od, ev, 0x07060302));
        }
      }
    }
  }
  f32x4 gt = ((const f32x4*)gate)[tid];
  __syncthreads();

  u32x4 A[16];
  {
    const int m = lane & 3, ep = (lane >> 2) & 3;
    lptr ka = kb + 32 + (-12 + 4 * m + 4 * (8 * q + 7 - ep)) * 2;
#pragma unroll
    for (int c = 0; c < 16; c++) {
      u32x2 lo = lds_tr(ka + c * 256), hi = lds_tr(ka + c * 256 - 32);
      A[c] = u32x4{lo[0], lo[1], hi[0], hi[1]};
    }
  }
  f32x4 acc[8];
#pragma unroll
  for (int h = 0; h < 8; h++) acc[h] = f32x4{0, 0, 0, 0};
  {
    lptr xb = smem + OFF_X + w * XB_STRIDE + (64 - 32 + 4 * j16 - 8 * q - 4) * 2;
    lptr xb2 = xb + 8;
    asm volatile("" : "+v"(xb2));
    const int rot = (lane == 0 ? 63 : q == 0 ? lane + 47 : lane - 16) * 4;
#pragma unroll
    for (int r = 3; r >= 0; r--) {
#pragma unroll
      for (int s = 0; s < 16; s++) {
        u32x2 lo = lds_r64(xb + r * XP_STRIDE + s * 64), hi = lds_r64(xb2 + r * XP_STRIDE + s * 64);
        u32x4 B = {lo[0], lo[1], hi[0], hi[1]};
#pragma unroll
        for (int h = 0; h < 8; h++) { int c = 2 * h - (s - 1); if (c >= 0 && c < 16) acc[h] = mfma(A[c], B, acc[h]); }
      }
      if (r > 0) {
        float t[8];
#pragma unroll
        for (int h = 0; h < 8; h++) t[h] = __int_as_float(__builtin_amdgcn_ds_bpermute(rot, __float_as_int(acc[h][3])));
#pragma unroll
        for (int h = 0; h < 8; h++) {
          float n0 = lane == 0 ? (h ? t[h - 1] : 0.f) : t[h];
          acc[h] = f32x4{n0, acc[h][0], acc[h][1], acc[h][2]};
        }
      }
    }
  }
#pragma unroll
  for (int h = 0; h < 8; h++)
    lds_w64(kb + 32 + (256 * h + 16 * j16 + 4 * q) * 2, u32x2{cvtpk(acc[h][0], acc[h][1]), cvtpk(acc[h][2], acc[h][3])});
  __syncthreads();

  u32x2 ya[4], yb[4];
#pragma unroll
  for (int c = 0; c < 4; c++) {
    ya[c] = lds_r64(smem + OFF_K + c * KB_STRIDE + 32 + tid * 8);
    yb[c] = lds_r64(smem + OFF_K + (4 + c) * KB_STRIDE + 32 + tid * 8);
  }
#pragma unroll
  for (int e = 0; e < 4; e++) {
    float g = gt[e], g1 = rb(1.f - g);
    u32x2 o;
#pragma unroll
    for (int cp = 0; cp < 2; cp++) {
      float res[2];
#pragma unroll
      for (int hl = 0; hl < 2; hl++) {
        int c = cp * 2 + hl;
        float uu = hl ? bhi(xu[e][cp]) : blo(xu[e][cp]);
        float kv = hl ? bhi(xkv[e][cp]) : blo(xkv[e][cp]);
        float d1 = hl ? bhi(dsk[cp]) : blo(dsk[cp]);
        float d2 = hl ? bhi(dss[cp]) : blo(dss[cp]);
        float y1 = (e & 1) ? bhi(ya[c][e >> 1]) : blo(ya[c][e >> 1]);
        float y2 = (e & 1) ? bhi(yb[c][e >> 1]) : blo(yb[c][e >> 1]);
        float a = rb(y1 + rb(uu * d1));
        float bb = rb(uu * rb(y2 + rb(kv * d2)));
        res[hl] = rb(g * a) + rb(g1 * bb);
      }
      o[cp] = cvtpk(res[0], res[1]);
    }
    out[(r0 + e) * 256 + cg] = o;
  }
}

extern "C" void kernel_launch(void* const* d_in, const int* in_sizes, int n_in, void* d_out, int out_size, void* d_ws,
                              size_t ws_size, hipStream_t stream) {
  float* g = (float*)d_ws;
  gate_kernel<<<256, 512, 0, stream>>>((const u32x4*)d_in[0], (const u32x4*)d_in[13], g);
  (void)hipFuncSetAttribute((const void*)conv_kernel, hipFuncAttributeMaxDynamicSharedMemorySize, LDS_BYTES);
  conv_kernel<<<256, 512, LDS_BYTES, stream>>>(
      (const u32x2*)d_in[0], (const u16*)d_in[1], (const u16*)d_in[2], (const u16*)d_in[3], (const u16*)d_in[4],
      (const u16*)d_in[5], (const u16*)d_in[6], (const u32x2*)d_in[8], (const u32x2*)d_in[9], (const u16*)d_in[10],
      (const u32x4*)d_in[11], (const u16*)d_in[12], g, (u32x2*)d_out);
}
```

```cpp
#include <hip/hip_runtime.h>
#include <hip/hip_bf16.h>

typedef unsigned int u32;
typedef unsigned short u16;
typedef unsigned long long u64;
typedef u32 u32x2 __attribute__((ext_vector_type(2)));
typedef u32 u32x4 __attribute__((ext_vector_type(4)));
typedef float f32x2 __attribute__((ext_vector_type(2)));
typedef float f32x4 __attribute__((ext_vector_type(4)));
typedef __bf16 bf16x2 __attribute__((ext_vector_type(2)));
typedef __bf16 bf16x8 __attribute__((ext_vector_type(8)));
typedef short s16x4 __attribute__((ext_vector_type(4)));
#define DI __device__ __forceinline__
#define LDS __attribute__((address_space(3)))

constexpr int L = 2048, D = 1024, NM = 128;
constexpr int KB_STRIDE = 4128;
constexpr int XP_STRIDE = 1184;
constexpr int XB_STRIDE = 4 * XP_STRIDE;
constexpr int TB_ROW = 528;
constexpr int TB_STRIDE = 16 * TB_ROW;
constexpr int OFF_K = 0;
constexpr int OFF_X = OFF_K + 8 * KB_STRIDE;
constexpr int OFF_T = OFF_X + 8 * XB_STRIDE;
constexpr int OFF_MISC = OFF_T + 4 * TB_STRIDE;
constexpr int OFF_PF = OFF_MISC + 16;
constexpr int LDS_BYTES = OFF_PF + 8192;
#define DLY1 16
#define PREFETCH 1

DI u32 cvtpk(float lo, float hi) { f32x2 v = {lo, hi}; bf16x2 b = __builtin_convertvector(v, bf16x2); return __builtin_bit_cast(u32, b); }
DI float blo(u32 x) { return __uint_as_float(x << 16); }
DI float bhi(u32 x) { return __uint_as_float(x & 0xffff0000u); }
DI float rb(float x) { return blo(cvtpk(x, x)); }
DI float bf(u16 x) { return __uint_as_float((u32)x << 16); }
DI f32x4 mfma(u32x4 a, u32x4 b, f32x4 c) {
  return __builtin_amdgcn_mfma_f32_16x16x32_bf16(__builtin_bit_cast(bf16x8, a), __builtin_bit_cast(bf16x8, b), c, 0, 0, 0);
}
typedef LDS char* lptr;
DI u32x2 lds_r64(lptr p) { return *(LDS u32x2*)p; }
DI u32x4 lds_r128(lptr p) { return *(LDS u32x4*)p; }
DI void lds_w32(lptr p, u32 v) { *(LDS u32*)p = v; }
DI void lds_w16(lptr p, u16 v) { *(LDS u16*)p = v; }
DI void lds_w64(lptr p, u32x2 v) { *(LDS u32x2*)p = v; }
DI void lds_w128(lptr p, u32x4 v) { *(LDS u32x4*)p = v; }
DI u32x2 lds_tr(lptr p) {
  typedef short v4i16_t __attribute__((ext_vector_type(4)));
  return __builtin_bit_cast(u32x2, __builtin_amdgcn_ds_read_tr16_b64_v4i16((LDS v4i16_t*)p));
}
DI void wsync() { __builtin_amdgcn_fence(__ATOMIC_ACQ_REL, "wavefront"); __builtin_amdgcn_wave_barrier(); }
DI u32 perm(u32 hi, u32 lo, u32 sel) { return __builtin_amdgcn_perm(hi, lo, sel); }

#define STAMP(i)
struct cplx { f32x2 r, i; };
DI cplx cmul(cplx a, cplx b) { cplx c; c.r = a.r * b.r - a.i * b.i; c.i = a.r * b.i + a.i * b.r; return c; }
DI cplx csq(cplx a) { cplx c; c.r = a.r * a.r - a.i * a.i; f32x2 t = a.r * a.i; c.i = t + t; return c; }
DI cplx conj(cplx a) { cplx c; c.r = a.r; c.i = -a.i; return c; }
DI cplx cexp_p(f32x2 zr2, f32x2 zi_rev, float p) {
  cplx c;
#pragma unroll
  for (int m = 0; m < 2; m++) {
    float mag = __builtin_amdgcn_exp2f(zr2[m] * p);
    float ph = __builtin_amdgcn_fractf(zi_rev[m] * p);
    c.r[m] = mag * __builtin_amdgcn_cosf(ph);
    c.i[m] = mag * __builtin_amdgcn_sinf(ph);
  }
  return c;
}
DI void tput(lptr trow, cplx e) { u32x2 v; v[0] = cvtpk(e.r[0], e.i[0]); v[1] = cvtpk(e.r[1], e.i[1]); lds_w64(trow, v); }

typedef __amdgpu_buffer_rsrc_t rsrc_t;
DI rsrc_t mkrsrc(const void* p, u32 bytes) { return __builtin_amdgcn_make_buffer_rsrc((void*)p, 0, bytes, 0x00020000); }
DI u32 bl16(rsrc_t r, u32 off) { return __builtin_amdgcn_raw_buffer_load_b16(r, off, 0, 0); }
DI u32 bl32(rsrc_t r, u32 off) { return __builtin_amdgcn_raw_buffer_load_b32(r, off, 0, 0); }
DI u32x2 bl64(rsrc_t r, u32 off) { return __builtin_bit_cast(u32x2, __builtin_amdgcn_raw_buffer_load_b64(r, off, 0, 0)); }
DI u32x4 bl128(rsrc_t r, u32 off) { return __builtin_bit_cast(u32x4, __builtin_amdgcn_raw_buffer_load_b128(r, off, 0, 0)); }
constexpr u32 OOB = 0x80000000u;

template <int OFF> DI void rdB(u32x2& lo, u32x2& hi, u32 a) {
  asm volatile("ds_read_b64 %0, %2 offset:%3\n\tds_read_b64 %1, %2 offset:%4" : "=&v"(lo), "=&v"(hi) : "v"(a), "i"(OFF), "i"(OFF + 8) : "memory");
}
struct BF { u32x2 lo[4], hi[4]; };
template <int G, int R> DI void loadB(BF& b, u32 xa) {
  rdB<R * XP_STRIDE + (4 * G + 0) * 64>(b.lo[0], b.hi[0], xa);
  rdB<R * XP_STRIDE + (4 * G + 1) * 64>(b.lo[1], b.hi[1], xa);
  rdB<R * XP_STRIDE + (4 * G + 2) * 64>(b.lo[2], b.hi[2], xa);
  rdB<R * XP_STRIDE + (4 * G + 3) * 64>(b.lo[3], b.hi[3], xa);
}
DI void waitB(BF& b) {
  asm volatile("s_waitcnt lgkmcnt(0)" : "+v"(b.lo[0]), "+v"(b.hi[0]), "+v"(b.lo[1]), "+v"(b.hi[1]), "+v"(b.lo[2]), "+v"(b.hi[2]), "+v"(b.lo[3]), "+v"(b.hi[3]) :: "memory");
}
template <int G> DI void phaseMM(const u32x4 (&A)[16], const BF& b, f32x4 (&acc)[8]) {
#pragma unroll
  for (int i = 0; i < 4; i++) {
    u32x4 B = {b.lo[i][0], b.lo[i][1], b.hi[i][0], b.hi[i][1]};
#pragma unroll
    for (int h = 2 * G; h < 8; h++) { int c = 2 * h - (4 * G + i - 1); if (c >= 0) acc[h] = mfma(A[c], B, acc[h]); }
  }
}
template <int G> DI void shift1(f32x4 (&acc)[8], int rot, bool l0) {
  float t[8];
#pragma unroll
  for (int h = 2 * G; h < 8; h++) t[h] = __int_as_float(__builtin_amdgcn_ds_bpermute(rot, __float_as_int(acc[h][3])));
#pragma unroll
  for (int h = 2 * G; h < 8; h++) {
    float n0 = l0 ? (h > 2 * G ? t[h - 1] : 0.f) : t[h];
    acc[h] = f32x4{n0, acc[h][0], acc[h][1], acc[h][2]};
  }
}

DI void process_x(lptr smem, int rowq, int lane, const u32x2 (&xu)[4], const u32x2 (&xk)[4], const u32x2 (&xv)[4],
                  const u32x2 (&hk)[3], u32x4 shw0, u32x4 shw1, u32x2 (&xkv)[4]) {
  u32x2 kr[7];
#pragma unroll
  for (int e = 0; e < 3; e++) {
    kr[e][0] = __builtin_amdgcn_update_dpp(hk[e][0], xk[e + 1][0], 0x138, 0xf, 0xf, false);
    kr[e][1] = __builtin_amdgcn_update_dpp(hk[e][1], xk[e + 1][1], 0x138, 0xf, 0xf, false);
  }
#pragma unroll
  for (int e = 0; e < 4; e++) kr[3 + e] = xk[e];
#pragma unroll
  for (int cp = 0; cp < 2; cp++) {
    u32x4 sw = cp ? shw1 : shw0;
    f32x2 t0 = {blo(sw[0]), blo(sw[2])}, t1 = {bhi(sw[0]), bhi(sw[2])}, t2 = {blo(sw[1]), blo(sw[3])}, t3 = {bhi(sw[1]), bhi(sw[3])};
    f32x2 kf[7];
#pragma unroll
    for (int e = 0; e < 7; e++) kf[e] = f32x2{blo(kr[e][cp]), bhi(kr[e][cp])};
#pragma unroll
    for (int e = 0; e < 4; e++) {
      f32x2 p3 = kf[e] * t3, p2 = kf[e + 1] * t2, p1 = kf[e + 2] * t1, p0 = kf[e + 3] * t0;
      u32 u3 = cvtpk(p3[0], p3[1]), u2 = cvtpk(p2[0], p2[1]), u1 = cvtpk(p1[0], p1[1]), u0 = cvtpk(p0[0], p0[1]);
      f32x2 s = f32x2{blo(u3), bhi(u3)} + f32x2{blo(u2), bhi(u2)};
      u32 us = cvtpk(s[0], s[1]);
      s = f32x2{blo(us), bhi(us)} + f32x2{blo(u1), bhi(u1)}; us = cvtpk(s[0], s[1]);
      s = f32x2{blo(us), bhi(us)} + f32x2{blo(u0), bhi(u0)}; us = cvtpk(s[0], s[1]);
      s = f32x2{blo(us), bhi(us)} * f32x2{blo(xv[e][cp]), bhi(xv[e][cp])};
      xkv[e][cp] = cvtpk(s[0], s[1]);
    }
  }
  const bool odd = lane & 1;
  lptr xw = smem + OFF_X + (odd ? 2 * XP_STRIDE : 0) + 128 + (rowq >> 1) * 4;
#pragma unroll
  for (int arr = 0; arr < 2; arr++) {
#pragma unroll
    for (int cp = 0; cp < 2; cp++) {
#pragma unroll
      for (int a = 0; a < 2; a++) {
        u32 own_keep = arr ? (odd ? xkv[2 + a][cp] : xkv[a][cp]) : (odd ? xu[2 + a][cp] : xu[a][cp]);
        u32 own_send = arr ? (odd ? xkv[a][cp] : xkv[2 + a][cp]) : (odd ? xu[a][cp] : xu[2 + a][cp]);
        u32 other = __builtin_amdgcn_update_dpp(0u, own_send, 0xb1, 0xf, 0xf, true);
        u32 ev = odd ? other : own_keep, od = odd ? own_keep : other;
        lds_w32(xw + (arr * 4 + cp * 2) * XB_STRIDE + a * XP_STRIDE, perm(od, ev, 0x05040100));
        lds_w32(xw + (arr * 4 + cp * 2 + 1) * XB_STRIDE + a * XP_STRIDE, perm(od, ev, 0x07060302));
      }
    }
  }
}

DI void epilogue(lptr smem, int rowq, int cg, u32 tag, const u64* gq, const u32x2 (&xu)[4], const u32x2 (&xkv)[4], u32x2 dsk,
                 u32x2 dss, rsrc_t rout) {
  u64 gr[4];
#pragma unroll
  for (int e = 0; e < 4; e++) gr[e] = __hip_atomic_load(gq + rowq * 4 + e, __ATOMIC_RELAXED, __HIP_MEMORY_SCOPE_AGENT);
  u32x2 ya[4], yb[4];
#pragma unroll
  for (int c = 0; c < 4; c++) {
    ya[c] = lds_r64(smem + OFF_K + c * KB_STRIDE + 32 + rowq * 8);
    yb[c] = lds_r64(smem + OFF_K + (4 + c) * KB_STRIDE + 32 + rowq * 8);
  }
  for (int it = 0; it < (1 << 16); it++) {
    bool ok = true;
#pragma unroll
    for (int e = 0; e < 4; e++) ok = ok && (u32)(gr[e] >> 32) == tag;
    if (__all(ok)) break;
    __builtin_amdgcn_s_sleep(2);
#pragma unroll
    for (int e = 0; e < 4; e++) gr[e] = __hip_atomic_load(gq + rowq * 4 + e, __ATOMIC_RELAXED, __HIP_MEMORY_SCOPE_AGENT);
  }
#pragma unroll
  for (int e = 0; e < 4; e++) {
    float g = __uint_as_float((u32)gr[e]), g1 = rb(1.f - g);
    u32x2 o;
#pragma unroll
    for (int cp = 0; cp < 2; cp++) {
      float res[2];
#pragma unroll
      for (int hl = 0; hl < 2; hl++) {
        int c = cp * 2 + hl;
        float uu = hl ? bhi(xu[e][cp]) : blo(xu[e][cp]);
        float kv = hl ? bhi(xkv[e][cp]) : blo(xkv[e][cp]);
        float d1 = hl ? bhi(dsk[cp]) : blo(dsk[cp]);
        float d2 = hl ? bhi(dss[cp]) : blo(dss[cp]);
        float y1 = (e & 1) ? bhi(ya[c][e >> 1]) : blo(ya[c][e >> 1]);
        float y2 = (e & 1) ? bhi(yb[c][e >> 1]) : blo(yb[c][e >> 1]);
        float a = rb(y1 + rb(uu * d1));
        float bb = rb(uu * rb(y2 + rb(kv * d2)));
        res[hl] = rb(g * a) + rb(g1 * bb);
      }
      o[cp] = cvtpk(res[0], res[1]);
    }
    __builtin_amdgcn_raw_buffer_store_b64(__builtin_bit_cast(__attribute__((ext_vector_type(2))) unsigned, o), rout, ((rowq * 4 + e) * 256 + cg) * 8, 0, 0);
  }
}

template <int G, class F> DI void segment(const u32x4 (&A)[16], f32x4 (&y)[8], u32 xa, lptr yw, int rot, bool l0, F mid) {
  f32x4 acc[8];
#pragma unroll
  for (int h = 2 * G; h < 8; h++) acc[h] = f32x4{0, 0, 0, 0};
  BF b0, b1;
  loadB<G, 3>(b0, xa);
  waitB(b0);
  loadB<G, 2>(b1, xa);
  phaseMM<G>(A, b0, acc); shift1<G>(acc, rot, l0);
  waitB(b1);
  loadB<G, 1>(b0, xa);
  phaseMM<G>(A, b1, acc); shift1<G>(acc, rot, l0);
  mid();
  waitB(b0);
  loadB<G, 0>(b1, xa);
  phaseMM<G>(A, b0, acc); shift1<G>(acc, rot, l0);
  waitB(b1);
  phaseMM<G>(A, b1, acc);
#pragma unroll
  for (int h = 2 * G; h < 8; h++) { if (G == 0) y[h] = acc[h]; else y[h] += acc[h]; }
#pragma unroll
  for (int h = 2 * G; h < 2 * G + 2; h++) lds_w64(yw + 512 * h, u32x2{cvtpk(y[h][0], y[h][1]), cvtpk(y[h][2], y[h][3])});
}

__global__ __launch_bounds__(512, 2) void conv_kernel(
    const void* __restrict__ gu, const void* __restrict__ logAr, const void* __restrict__ Aim, const void* __restrict__ Cre,
    const void* __restrict__ Cim, const void* __restrict__ logdt, const void* __restrict__ Dskip, const void* __restrict__ gk,
    const void* __restrict__ gv, const void* __restrict__ shk, const void* __restrict__ ssmk, const void* __restrict__ ssmD,
    const void* __restrict__ gatew, void* ws, void* __restrict__ out) {
  extern __shared__ __attribute__((aligned(16))) char smem_[];
  lptr smem = (lptr)smem_;
  const int b = blockIdx.x, cg = (b & 7) * 32 + (b >> 3);
  const int tid = threadIdx.x, lane = tid & 63, w = __builtin_amdgcn_readfirstlane(tid >> 6);
  const int ch = cg * 4 + (w & 3);
  const int j16 = lane & 15, q = lane >> 4;
  const bool isA = w < 4;
  lptr kb = smem + OFF_K + w * KB_STRIDE;
  u64* gq = (u64*)ws;
  u32* gepoch = (u32*)((char*)ws + 65536);
  const rsrc_t ru = mkrsrc(gu, L * D * 2), rk = mkrsrc(gk, L * D * 2), rv = mkrsrc(gv, L * D * 2), rout = mkrsrc(out, L * D * 2);
  STAMP(0);
  const int rowq = (tid + 256) & 511;
  const u32 xoff = (rowq * 4 * 256 + cg) * 8;
  u32x2 xu[4], xk[4], xv[4], hk[3], xkv[4];
  auto load_x = [&]() {
#pragma unroll
    for (int e = 0; e < 4; e++) xu[e] = bl64(ru, xoff + e * 2048);
#pragma unroll
    for (int e = 0; e < 4; e++) xk[e] = bl64(rk, xoff + e * 2048);
#pragma unroll
    for (int e = 0; e < 3; e++) hk[e] = bl64(rk, (lane == 0 && rowq > 0) ? xoff - (3 - e) * 2048 : OOB);
#pragma unroll
    for (int e = 0; e < 4; e++) xv[e] = bl64(rv, xoff + e * 2048);
  };
  auto prefetch = [&]() {
    const u32 po = ((b >> 3) * 64 + (tid >> 4)) * 2048 + (b & 7) * 256 + (tid & 15) * 16;
    LDS void* sink = (LDS void*)(smem + OFF_PF + w * 1024);
    __builtin_amdgcn_raw_ptr_buffer_load_lds(rk, sink, 16, po, 0, 0, 0);
    __builtin_amdgcn_raw_ptr_buffer_load_lds(rv, sink, 16, po, 0, 0, 0);
    __builtin_amdgcn_raw_ptr_buffer_load_lds(ru, sink, 16, po, 0, 0, 0);
    __builtin_amdgcn_raw_ptr_buffer_load_lds(rk, sink, 16, po + 32 * 2048, 0, 0, 0);
    __builtin_amdgcn_raw_ptr_buffer_load_lds(rv, sink, 16, po + 32 * 2048, 0, 0, 0);
    __builtin_amdgcn_raw_ptr_buffer_load_lds(ru, sink, 16, po + 32 * 2048, 0, 0, 0);
  };
  const u32x4 shw0 = ((const u32x4*)shk)[cg * 2], shw1 = ((const u32x4*)shk)[cg * 2 + 1];
  const u32x2 dsk = ((const u32x2*)Dskip)[cg], dss = ((const u32x2*)ssmD)[cg];

  if (isA) {
    const rsrc_t r1 = mkrsrc(logAr, D * NM * 2), r2 = mkrsrc(Aim, D * NM * 2), r3 = mkrsrc(Cre, D * NM * 2), r4 = mkrsrc(Cim, D * NM * 2);
    const u32 po = (ch * 64 + lane) * 4;
    u32 pAr = bl32(r1, po), pAi = bl32(r2, po), pCr = bl32(r3, po), pCi = bl32(r4, po);
    u32 pdt = ((const u16*)logdt)[ch];
    prefetch();
    float dt = rb(expf(bf(pdt)));
    f32x2 zr, zi; cplx C;
    zr[0] = -rb(expf(blo(pAr))) * dt; zr[1] = -rb(expf(bhi(pAr))) * dt;
    zi[0] = blo(pAi) * dt; zi[1] = bhi(pAi) * dt;
    C.r[0] = 2.f * blo(pCr) * dt; C.r[1] = 2.f * bhi(pCr) * dt;
    C.i[0] = -2.f * blo(pCi) * dt; C.i[1] = -2.f * bhi(pCi) * dt;
    zr *= 1.44269504088896341f; zi *= 0.15915494309189535f;
    lptr tb = smem + OFF_T + w * TB_STRIDE;
    lptr tw = tb + lane * 8, tr = tb + j16 * TB_ROW + q * 16;
    cplx w1 = cexp_p(zr, zi, 1.f), w16 = cexp_p(zr, zi, 16.f), w256 = cexp_p(zr, zi, 256.f);
    u32x4 G[8];
    {
      cplx g; g.r = f32x2{1.f, 1.f}; g.i = f32x2{0.f, 0.f};
      tput(tw, g); g = w16; tput(tw + TB_ROW, g);
#pragma unroll
      for (int i = 2; i < 16; i++) { g = cmul(g, w16); tput(tw + i * TB_ROW, g); }
      wsync();
#pragma unroll
      for (int s = 0; s < 8; s++) G[s] = lds_r128(tr + s * 64);
      wsync();
    }
    if (w < 2) load_x();
    w1 = conj(w1); w256 = conj(w256);
    cplx w2 = csq(w1), w4 = csq(w2), w8 = csq(w4), w512 = csq(w256), w1024 = csq(w512);
    lptr kd = kb + 32;
    if (lane < 8) lds_w32(kb + lane * 4, 0);
    {
      cplx e = C; tput(tw, e);
#pragma unroll
      for (int i = 1; i < 16; i++) { e = cmul(e, w1); tput(tw + i * TB_ROW, e); }
      f32x4 a = {0, 0, 0, 0};
      wsync();
#pragma unroll
      for (int s = 0; s < 8; s++) a = mfma(lds_r128(tr + s * 64), G[s], a);
      wsync();
      lds_w64(kd + (16 * j16 + 4 * q) * 2, u32x2{cvtpk(a[0], a[1]), cvtpk(a[2], a[3])});
    }
    if (w >= 2) load_x();
    {
      cplx e = cmul(C, w256); tput(tw, e);
#pragma unroll
      for (int i = 1; i < 8; i++) { e = cmul(e, w2); tput(tw + i * TB_ROW, e); }
      e = cmul(C, w512);
#pragma unroll
      for (int i = 0; i < 4; i++) { tput(tw + (8 + i) * TB_ROW, e); tput(tw + (12 + i) * TB_ROW, cmul(e, w256)); if (i < 3) e = cmul(e, w4); }
      f32x4 a = {0, 0, 0, 0};
      wsync();
#pragma unroll
      for (int s = 0; s < 8; s++) a = mfma(lds_r128(tr + s * 64), G[s], a);
      wsync();
      u32 p01 = cvtpk(a[0], a[1]), p23 = cvtpk(a[2], a[3]);
      u32 v00 = perm(p01, p01, 0x01000100), v11 = perm(p01, p01, 0x03020302), v22 = perm(p23, p23, 0x01000100), v33 = perm(p23, p23, 0x03020302);
      const int base = q < 2 ? 256 + 16 * j16 + 8 * q : 512 + 256 * (q - 2) + 16 * j16;
      lds_w128(kd + base * 2, q < 2 ? u32x4{v00, perm(p23, p01, 0x05040302), v22, v33} : u32x4{v00, p01, v11, v22});
      if (q >= 2) lds_w128(kd + base * 2 + 16, u32x4{v22, p23, v33, v33});
      wsync();
      if (q >= 2 && base > 512) lds_w32(kd + (base - 2) * 2, v00); else if (base > 256) lds_w16(kd + (base - 1) * 2, (u16)p01);
      wsync();
    }
    {
      cplx e0 = cmul(C, w1024), e1 = cmul(e0, w8);
#pragma unroll
      for (int i = 0; i < 4; i++) { tput(tw + (2 * i) * TB_ROW, e0); tput(tw + (2 * i + 1) * TB_ROW, e1); e0 = cmul(e0, w256); if (i < 3) e1 = cmul(e1, w256); }
      tput(tw + 8 * TB_ROW, e0);
      f32x4 a = {0, 0, 0, 0};
      wsync();
#pragma unroll
      for (int s = 0; s < 8; s++) a = mfma(lds_r128(tr + s * 64), G[s], a);
      wsync();
      u32 p01 = cvtpk(a[0], a[1]), p23 = cvtpk(a[2], a[3]);
      if (q < 2) {
#pragma unroll
        for (int hh = 0; hh < 2; hh++) {
          u32 p = hh ? p23 : p01;
          u32 v00 = perm(p, p, 0x01000100), v11 = perm(p, p, 0x03020302);
          int base = 1024 + 512 * q + 256 * hh + 16 * j16;
          lds_w128(kd + base * 2, u32x4{v00, v00, p, v11});
          lds_w128(kd + base * 2 + 16, u32x4{v11, v11, v11, v11});
        }
      }
      wsync();
      if (q < 2) {
#pragma unroll
        for (int hh = 0; hh < 2; hh++) {
          u32 p = hh ? p23 : p01;
          u32 v00 = perm(p, p, 0x01000100);
          int base = 1024 + 512 * q + 256 * hh + 16 * j16;
          if (base > 1024) lds_w64(kd + (base - 4) * 2, u32x2{v00, v00}); else lds_w32(kd + (base - 2) * 2, v00);
        }
      } else if (lane == 32) {
        u32 v00 = perm(p01, p01, 0x01000100);
        lds_w64(kd + 2044 * 2, u32x2{v00, v00});
      }
      wsync();
    }
  } else {
    const rsrc_t rs = mkrsrc(ssmk, L * D * 2), rw = mkrsrc(gatew, D * 2);
    u32x4 kk[4];
#pragma unroll
    for (int e = 0; e < 4; e++) kk[e] = bl128(rs, ch * 4096 + (e * 64 + lane) * 16);
    const u32 epoch = __hip_atomic_load(gepoch, __ATOMIC_RELAXED, __HIP_MEMORY_SCOPE_AGENT);
    const int grow = b * 8 + (w - 4) * 2;
    u32x4 gx[4], gw[2];
#pragma unroll
    for (int e = 0; e < 4; e++) gx[e] = bl128(ru, grow * 2048 + (e * 64 + lane) * 16);
#pragma unroll
    for (int e = 0; e < 2; e++) gw[e] = bl128(rw, (e * 64 + lane) * 16);
    prefetch();
    if (w >= 6) { for (int i = 0; i < DLY1; i++) __builtin_amdgcn_s_sleep(1); }
    load_x();
    for (int i = tid - 256; i < 32 * 32; i += 256) lds_w32(smem + OFF_X + (i >> 5) * XP_STRIDE + (i & 31) * 4, 0);
    if (lane < 8) lds_w32(kb + lane * 4, 0);
#pragma unroll
    for (int rr = 0; rr < 2; rr++) {
      float s = 0.f;
#pragma unroll
      for (int h = 0; h < 2; h++) {
        u32x4 x = gx[rr * 2 + h], ww = gw[h];
#pragma unroll
        for (int e = 0; e < 4; e++) { u32 p = cvtpk(blo(x[e]) * blo(ww[e]), bhi(x[e]) * bhi(ww[e])); s += blo(p) + bhi(p); }
      }
#pragma unroll
      for (int o = 32; o >= 1; o >>= 1) s += __shfl_xor(s, o);
      float z = rb(s), g = rb(1.f / (1.f + expf(-z)));
      if (lane == 0) __hip_atomic_store(gq + grow + rr, ((u64)(epoch + 1) << 32) | __float_as_uint(g), __ATOMIC_RELAXED, __HIP_MEMORY_SCOPE_AGENT);
    }
    if (tid == 256) lds_w32(smem + OFF_MISC, epoch + 1);
#pragma unroll
    for (int e = 0; e < 4; e++) lds_w128(kb + 32 + (e * 64 + lane) * 16, kk[e]);
    if (w < 6) process_x(smem, rowq, lane, xu, xk, xv, hk, shw0, shw1, xkv);
  }
  STAMP(1);
  __syncthreads();
  STAMP(2);
  const u32 tag = *(LDS u32*)(smem + OFF_MISC);

  u32x4 A[16];
  {
    const int m = lane & 3, ep = (lane >> 2) & 3;
    lptr ka = kb + 32 + (-12 + 4 * m + 4 * (8 * q + 7 - ep)) * 2;
#pragma unroll
    for (int c = 0; c < 16; c++) {
      u32x2 lo = lds_tr(ka + c * 256), hi = lds_tr(ka + c * 256 - 32);
      A[c] = u32x4{lo[0], lo[1], hi[0], hi[1]};
    }
  }
  const u32 xa = (u32)(size_t)(smem + OFF_X + w * XB_STRIDE + (64 - 32 + 4 * j16 - 8 * q - 4) * 2);
  const int rot = (lane == 0 ? 63 : q == 0 ? lane + 47 : lane - 16) * 4;
  lptr yw = kb + 32 + (16 * j16 + 4 * q) * 2;
  const int myseg = ((w + 4) & 7) >> 1;
  f32x4 y[8];
  segment<0>(A, y, xa, yw, rot, lane == 0, [&]() { if (myseg == 1) process_x(smem, rowq, lane, xu, xk, xv, hk, shw0, shw1, xkv); });
  STAMP(3);
  __syncthreads();
  if (myseg == 0) epilogue(smem, rowq, cg, tag, gq, xu, xkv, dsk, dss, rout);
  segment<1>(A, y, xa, yw, rot, lane == 0, [&]() { if (myseg == 2) process_x(smem, rowq, lane, xu, xk, xv, hk, shw0, shw1, xkv); });
  STAMP(4);
  __syncthreads();
  if (myseg == 1) epilogue(smem, rowq, cg, tag, gq, xu, xkv, dsk, dss, rout);
  segment<2>(A, y, xa, yw, rot, lane == 0, [&]() { if (myseg == 3) process_x(smem, rowq, lane, xu, xk, xv, hk, shw0, shw1, xkv); });
  STAMP(5);
  __syncthreads();
  if (myseg == 2) epilogue(smem, rowq, cg, tag, gq, xu, xkv, dsk, dss, rout);
  segment<3>(A, y, xa, yw, rot, lane == 0, [&]() {});
  STAMP(6);
  __syncthreads();
  if (myseg == 3) epilogue(smem, rowq, cg, tag, gq, xu, xkv, dsk, dss, rout);
  if (b == 0) { __syncthreads(); if (tid == 0) __hip_atomic_store(gepoch, tag, __ATOMIC_RELAXED, __HIP_MEMORY_SCOPE_AGENT); }
  STAMP(7);
}

extern "C" void kernel_launch(void* const* d_in, const int* in_sizes, int n_in, void* d_out, int out_size, void* d_ws,
                              size_t ws_size, hipStream_t stream) {
  (void)hipFuncSetAttribute((const void*)conv_kernel, hipFuncAttributeMaxDynamicSharedMemorySize, LDS_BYTES);
  conv_kernel<<<256, 512, LDS_BYTES, stream>>>(d_in[0], d_in[1], d_in[2], d_in[3], d_in[4], d_in[5], d_in[6], d_in[8], d_in[9],
                                               d_in[10], d_in[11], d_in[12], d_in[13], d_ws, d_out);
}
```

```cpp
#include <hip/hip_runtime.h>
#include <hip/hip_bf16.h>

typedef unsigned int u32;
typedef unsigned short u16;
typedef unsigned long long u64;
typedef u32 u32x2 __attribute__((ext_vector_type(2)));
typedef u32 u32x4 __attribute__((ext_vector_type(4)));
typedef float f32x2 __attribute__((ext_vector_type(2)));
typedef float f32x4 __attribute__((ext_vector_type(4)));
typedef __bf16 bf16x2 __attribute__((ext_vector_type(2)));
typedef __bf16 bf16x8 __attribute__((ext_vector_type(8)));
typedef short s16x4 __attribute__((ext_vector_type(4)));
#define DI __device__ __forceinline__
#define LDS __attribute__((address_space(3)))

constexpr int L = 2048, D = 1024, NM = 128;
constexpr int KB_STRIDE = 4128;
constexpr int XP_STRIDE = 1184;
constexpr int XB_STRIDE = 4 * XP_STRIDE + 64;
constexpr int TB_ROW = 528;
constexpr int TB_STRIDE = 16 * TB_ROW;
constexpr int OFF_K = 0;
constexpr int OFF_X = OFF_K + 8 * KB_STRIDE;
constexpr int OFF_T = OFF_X + 8 * XB_STRIDE;
constexpr int OFF_MISC = OFF_T + 4 * TB_STRIDE;
constexpr int OFF_PF = OFF_MISC + 64;
constexpr int LDS_BYTES = OFF_PF + 4096;
#define PREFETCH 1

DI u32 cvtpk(float lo, float hi) { f32x2 v = {lo, hi}; bf16x2 b = __builtin_convertvector(v, bf16x2); return __builtin_bit_cast(u32, b); }
DI float blo(u32 x) { return __uint_as_float(x << 16); }
DI float bhi(u32 x) { return __uint_as_float(x & 0xffff0000u); }
DI float rb(float x) { return blo(cvtpk(x, x)); }
DI float bf(u16 x) { return __uint_as_float((u32)x << 16); }
DI f32x4 mfma(u32x4 a, u32x4 b, f32x4 c) {
  return __builtin_amdgcn_mfma_f32_16x16x32_bf16(__builtin_bit_cast(bf16x8, a), __builtin_bit_cast(bf16x8, b), c, 0, 0, 0);
}
typedef LDS char* lptr;
DI u32x2 lds_r64(lptr p) { return *(LDS u32x2*)p; }
DI u32x4 lds_r128(lptr p) { return *(LDS u32x4*)p; }
DI void lds_w32(lptr p, u32 v) { *(LDS u32*)p = v; }
DI void lds_w16(lptr p, u16 v) { *(LDS u16*)p = v; }
DI void lds_w64(lptr p, u32x2 v) { *(LDS u32x2*)p = v; }
DI void lds_w128(lptr p, u32x4 v) { *(LDS u32x4*)p = v; }
DI u32x2 lds_tr(lptr p) {
  typedef short v4i16_t __attribute__((ext_vector_type(4)));
  return __builtin_bit_cast(u32x2, __builtin_amdgcn_ds_read_tr16_b64_v4i16((LDS v4i16_t*)p));
}
DI void wsync() { __builtin_amdgcn_fence(__ATOMIC_ACQ_REL, "wavefront"); __builtin_amdgcn_wave_barrier(); }
DI u32 perm(u32 hi, u32 lo, u32 sel) { return __builtin_amdgcn_perm(hi, lo, sel); }

#define STAMP(i)
struct cplx { f32x2 r, i; };
DI cplx cmul(cplx a, cplx b) { cplx c; c.r = a.r * b.r - a.i * b.i; c.i = a.r * b.i + a.i * b.r; return c; }
DI cplx csq(cplx a) { cplx c; c.r = a.r * a.r - a.i * a.i; f32x2 t = a.r * a.i; c.i = t + t; return c; }
DI cplx conj(cplx a) { cplx c; c.r = a.r; c.i = -a.i; return c; }
DI cplx cexp_p(f32x2 zr2, f32x2 zi_rev, float p) {
  cplx c;
#pragma unroll
  for (int m = 0; m < 2; m++) {
    float mag = __builtin_amdgcn_exp2f(zr2[m] * p);
    float ph = __builtin_amdgcn_fractf(zi_rev[m] * p);
    c.r[m] = mag * __builtin_amdgcn_cosf(ph);
    c.i[m] = mag * __builtin_amdgcn_sinf(ph);
  }
  return c;
}
DI void tput(lptr trow, cplx e) { u32x2 v; v[0] = cvtpk(e.r[0], e.i[0]); v[1] = cvtpk(e.r[1], e.i[1]); lds_w64(trow, v); }

typedef __amdgpu_buffer_rsrc_t rsrc_t;
DI rsrc_t mkrsrc(const void* p, u32 bytes) { return __builtin_amdgcn_make_buffer_rsrc((void*)p, 0, bytes, 0x00020000); }
DI u32 bl16(rsrc_t r, u32 off) { return __builtin_amdgcn_raw_buffer_load_b16(r, off, 0, 0); }
DI u32 bl32(rsrc_t r, u32 off) { return __builtin_amdgcn_raw_buffer_load_b32(r, off, 0, 0); }
DI u32x2 bl64(rsrc_t r, u32 off) { return __builtin_bit_cast(u32x2, __builtin_amdgcn_raw_buffer_load_b64(r, off, 0, 0)); }
DI u32x4 bl128(rsrc_t r, u32 off) { return __builtin_bit_cast(u32x4, __builtin_amdgcn_raw_buffer_load_b128(r, off, 0, 0)); }
constexpr u32 OOB = 0x80000000u;

template <int OFF> DI void rdB(u32x2& lo, u32x2& hi, u32 a) {
  asm volatile("ds_read_b64 %0, %2 offset:%3\n\tds_read_b64 %1, %2 offset:%4" : "=&v"(lo), "=&v"(hi) : "v"(a), "i"(OFF), "i"(OFF + 8) : "memory");
}
struct BF { u32x2 lo[4], hi[4]; };
template <int G, int R> DI void loadB(BF& b, u32 xa) {
  rdB<R * XP_STRIDE + (4 * G + 0) * 64>(b.lo[0], b.hi[0], xa);
  rdB<R * XP_STRIDE + (4 * G + 1) * 64>(b.lo[1], b.hi[1], xa);
  rdB<R * XP_STRIDE + (4 * G + 2) * 64>(b.lo[2], b.hi[2], xa);
  rdB<R * XP_STRIDE + (4 * G + 3) * 64>(b.lo[3], b.hi[3], xa);
}
DI void waitB(BF& b) {
  asm volatile("s_waitcnt lgkmcnt(0)" : "+v"(b.lo[0]), "+v"(b.hi[0]), "+v"(b.lo[1]), "+v"(b.hi[1]), "+v"(b.lo[2]), "+v"(b.hi[2]), "+v"(b.lo[3]), "+v"(b.hi[3]) :: "memory");
}
template <int G> DI void phaseMM(const u32x4 (&A)[16], const BF& b, f32x4 (&acc)[8]) {
#pragma unroll
  for (int i = 0; i < 4; i++) {
    u32x4 B = {b.lo[i][0], b.lo[i][1], b.hi[i][0], b.hi[i][1]};
#pragma unroll
    for (int h = 2 * G; h < 8; h++) { int c = 2 * h - (4 * G + i - 1); if (c >= 0) acc[h] = mfma(A[c], B, acc[h]); }
  }
}
template <int G> DI void shift1(f32x4 (&acc)[8], int rot, bool l0) {
  float t[8];
#pragma unroll
  for (int h = 2 * G; h < 8; h++) t[h] = __int_as_float(__builtin_amdgcn_ds_bpermute(rot, __float_as_int(acc[h][3])));
#pragma unroll
  for (int h = 2 * G; h < 8; h++) {
    float n0 = l0 ? (h > 2 * G ? t[h - 1] : 0.f) : t[h];
    acc[h] = f32x4{n0, acc[h][0], acc[h][1], acc[h][2]};
  }
}

DI void cnt_signal(lptr p, int lane) {
  asm volatile("s_waitcnt lgkmcnt(0)" ::: "memory");
  if (lane == 0) __hip_atomic_fetch_add((LDS u32*)p, 1u, __ATOMIC_RELAXED, __HIP_MEMORY_SCOPE_WORKGROUP);
  asm volatile("" ::: "memory");
}
DI void cnt_wait(lptr p, u32 target) {
  asm volatile("" ::: "memory");
  for (int it = 0; it < (1 << 22); it++) {
    u32 v = __builtin_amdgcn_readfirstlane(__hip_atomic_load((LDS u32*)p, __ATOMIC_RELAXED, __HIP_MEMORY_SCOPE_WORKGROUP));
    if (v >= target) break;
    __builtin_amdgcn_s_sleep(1);
  }
  asm volatile("" ::: "memory");
}

struct XIn { u32x2 k[2], v[2], h; };
DI void process_x(lptr xw, u32 sel0, u32 sel1, bool odd2, const u32x2 (&xu)[2], const XIn& in, u32x4 shw0, u32x4 shw1,
                  u32x2 (&xkv)[2]) {
  u32x2 kr[5];
#pragma unroll
  for (int d = 0; d < 2; d++) {
    u32 h1 = __builtin_amdgcn_update_dpp(0u, in.h[d], 0x101, 0xf, 0xf, true);
    u32 h2 = __builtin_amdgcn_update_dpp(0u, in.h[d], 0x102, 0xf, 0xf, true);
    kr[1][d] = __builtin_amdgcn_update_dpp(h1, in.k[0][d], 0x138, 0xf, 0xf, false);
    kr[2][d] = __builtin_amdgcn_update_dpp(h2, in.k[1][d], 0x138, 0xf, 0xf, false);
    kr[0][d] = __builtin_amdgcn_update_dpp(in.h[d], kr[2][d], 0x138, 0xf, 0xf, false);
    kr[3][d] = in.k[0][d]; kr[4][d] = in.k[1][d];
  }
#pragma unroll
  for (int cp = 0; cp < 2; cp++) {
    u32x4 sw = cp ? shw1 : shw0;
    f32x2 t0 = {blo(sw[0]), blo(sw[2])}, t1 = {bhi(sw[0]), bhi(sw[2])}, t2 = {blo(sw[1]), blo(sw[3])}, t3 = {bhi(sw[1]), bhi(sw[3])};
    f32x2 kf[5];
#pragma unroll
    for (int e = 0; e < 5; e++) kf[e] = f32x2{blo(kr[e][cp]), bhi(kr[e][cp])};
#pragma unroll
    for (int e = 0; e < 2; e++) {
      f32x2 p3 = kf[e] * t3, p2 = kf[e + 1] * t2, p1 = kf[e + 2] * t1, p0 = kf[e + 3] * t0;
      u32 u3 = cvtpk(p3[0], p3[1]), u2 = cvtpk(p2[0], p2[1]), u1 = cvtpk(p1[0], p1[1]), u0 = cvtpk(p0[0], p0[1]);
      f32x2 s = f32x2{blo(u3), bhi(u3)} + f32x2{blo(u2), bhi(u2)};
      u32 us = cvtpk(s[0], s[1]);
      s = f32x2{blo(us), bhi(us)} + f32x2{blo(u1), bhi(u1)}; us = cvtpk(s[0], s[1]);
      s = f32x2{blo(us), bhi(us)} + f32x2{blo(u0), bhi(u0)}; us = cvtpk(s[0], s[1]);
      s = f32x2{blo(us), bhi(us)} * f32x2{blo(in.v[e][cp]), bhi(in.v[e][cp])};
      xkv[e][cp] = cvtpk(s[0], s[1]);
    }
  }
#pragma unroll
  for (int arr = 0; arr < 2; arr++) {
#pragma unroll
    for (int a = 0; a < 2; a++) {
      u32x2 x = arr ? xkv[a] : xu[a];
      u32 keep = odd2 ? x[1] : x[0], send = odd2 ? x[0] : x[1];
      u32 other = __builtin_amdgcn_update_dpp(0u, send, 0x4e, 0xf, 0xf, true);
      lds_w32(xw + arr * 4 * XB_STRIDE + a * XP_STRIDE, perm(other, keep, sel0));
      lds_w32(xw + (arr * 4 + 1) * XB_STRIDE + a * XP_STRIDE, perm(other, keep, sel1));
    }
  }
}

DI void epilogue(lptr yr, u32 ooff, u32 tag, const u64* gp, u64 gr, const u32x2 (&xu)[2], const u32x2 (&xkv)[2], u32x2 dsk,
                 u32x2 dss, rsrc_t rout) {
  u32 ya[4], yb[4];
#pragma unroll
  for (int c = 0; c < 4; c++) {
    ya[c] = *(LDS u32*)(yr + c * KB_STRIDE);
    yb[c] = *(LDS u32*)(yr + (4 + c) * KB_STRIDE);
  }
  for (int it = 0; it < (1 << 16); it++) {
    if (__all((u32)(gr >> 32) == tag)) break;
    __builtin_amdgcn_s_sleep(2);
    gr = __hip_atomic_load(gp, __ATOMIC_RELAXED, __HIP_MEMORY_SCOPE_AGENT);
  }
#pragma unroll
  for (int e = 0; e < 2; e++) {
    float g = e ? bhi((u32)gr) : blo((u32)gr), g1 = rb(1.f - g);
    u32x2 o;
#pragma unroll
    for (int cp = 0; cp < 2; cp++) {
      float res[2];
#pragma unroll
      for (int hl = 0; hl < 2; hl++) {
        int c = cp * 2 + hl;
        float uu = hl ? bhi(xu[e][cp]) : blo(xu[e][cp]);
        float kv = hl ? bhi(xkv[e][cp]) : blo(xkv[e][cp]);
        float d1 = hl ? bhi(dsk[cp]) : blo(dsk[cp]);
        float d2 = hl ? bhi(dss[cp]) : blo(dss[cp]);
        float y1 = e ? bhi(ya[c]) : blo(ya[c]);
        float y2 = e ? bhi(yb[c]) : blo(yb[c]);
        float a = rb(y1 + rb(uu * d1));
        float bb = rb(uu * rb(y2 + rb(kv * d2)));
        res[hl] = rb(g * a) + rb(g1 * bb);
      }
      o[cp] = cvtpk(res[0], res[1]);
    }
    __builtin_amdgcn_raw_buffer_store_b64(__builtin_bit_cast(__attribute__((ext_vector_type(2))) unsigned, o), rout, ooff + e * 2048, 0, 0);
  }
}

template <int G, class F> DI void segment(const u32x4 (&A)[16], f32x4 (&y)[8], u32 xa, lptr yw, int rot, bool l0, lptr cnt, u32 xtarget, int lane, F mid) {
  cnt_wait(cnt + 4 * G, xtarget);
  f32x4 acc[8];
#pragma unroll
  for (int h = 2 * G; h < 8; h++) acc[h] = f32x4{0, 0, 0, 0};
  BF b0, b1;
  loadB<G, 3>(b0, xa);
  waitB(b0);
  loadB<G, 2>(b1, xa);
  phaseMM<G>(A, b0, acc); shift1<G>(acc, rot, l0);
  waitB(b1);
  loadB<G, 1>(b0, xa);
  phaseMM<G>(A, b1, acc); shift1<G>(acc, rot, l0);
  mid();
  waitB(b0);
  loadB<G, 0>(b1, xa);
  phaseMM<G>(A, b0, acc); shift1<G>(acc, rot, l0);
  waitB(b1);
  phaseMM<G>(A, b1, acc);
#pragma unroll
  for (int h = 2 * G; h < 8; h++) { if (G == 0) y[h] = acc[h]; else y[h] += acc[h]; }
#pragma unroll
  for (int h = 2 * G; h < 2 * G + 2; h++) lds_w64(yw + 512 * h, u32x2{cvtpk(y[h][0], y[h][1]), cvtpk(y[h][2], y[h][3])});
  cnt_signal(cnt + 16 + 4 * G, lane);
}

__global__ __launch_bounds__(512, 2) void conv_kernel(
    const void* __restrict__ gu, const void* __restrict__ logAr, const void* __restrict__ Aim, const void* __restrict__ Cre,
    const void* __restrict__ Cim, const void* __restrict__ logdt, const void* __restrict__ Dskip, const void* __restrict__ gk,
    const void* __restrict__ gv, const void* __restrict__ shk, const void* __restrict__ ssmk, const void* __restrict__ ssmD,
    const void* __restrict__ gatew, void* ws, void* __restrict__ out) {
  extern __shared__ __attribute__((aligned(16))) char smem_[];
  lptr smem = (lptr)smem_;
  const int b = blockIdx.x, cg = (b & 7) * 32 + (b >> 3);
  const int tid = threadIdx.x, lane = tid & 63, w = __builtin_amdgcn_readfirstlane(tid >> 6);
  const int ch = cg * 4 + (w & 3);
  const int j16 = lane & 15, q = lane >> 4;
  const bool isA = w < 4;
  lptr kb = smem + OFF_K + w * KB_STRIDE;
  u64* gq = (u64*)ws;
  u32* gepoch = (u32*)((char*)ws + 65536);
  const rsrc_t ru = mkrsrc(gu, L * D * 2), rk = mkrsrc(gk, L * D * 2), rv = mkrsrc(gv, L * D * 2), rout = mkrsrc(out, L * D * 2);
  STAMP(0);
  lptr cnt = smem + OFF_MISC;
  if (tid < 16) lds_w32(cnt + tid * 4, 0);
  auto loadA = [&](u32x4 (&A)[16]) {
    const int m = lane & 3, ep = (lane >> 2) & 3;
    lptr ka = kb + 32 + (-12 + 4 * m + 4 * (8 * q + 7 - ep)) * 2;
#pragma unroll
    for (int c = 0; c < 16; c++) {
      u32x2 lo = lds_tr(ka + c * 256), hi = lds_tr(ka + c * 256 - 32);
      A[c] = u32x4{lo[0], lo[1], hi[0], hi[1]};
    }
  };
  const u32 xa = (u32)(size_t)(smem + OFF_X + w * XB_STRIDE + (64 - 32 + 4 * j16 - 8 * q - 4) * 2);
  const int rot = (lane == 0 ? 63 : q == 0 ? lane + 47 : lane - 16) * 4;
  lptr yw = kb + 32 + (16 * j16 + 4 * q) * 2;
  u32x4 A[16];
  f32x4 y[8];

  if (isA) {
    const rsrc_t r1 = mkrsrc(logAr, D * NM * 2), r2 = mkrsrc(Aim, D * NM * 2), r3 = mkrsrc(Cre, D * NM * 2), r4 = mkrsrc(Cim, D * NM * 2);
    const u32 po = (ch * 64 + lane) * 4;
    u32 pAr = bl32(r1, po), pAi = bl32(r2, po), pCr = bl32(r3, po), pCi = bl32(r4, po);
    u32 pdt = ((const u16*)logdt)[ch];
    asm volatile("s_waitcnt lgkmcnt(0)\n\ts_barrier" ::: "memory");
    {
      const u32 pf = ((b >> 3) * 64 + (tid >> 4)) * 2048 + (b & 7) * 256 + (tid & 15) * 16;
      LDS void* sink = (LDS void*)(smem + OFF_PF + w * 1024);
#pragma unroll
      for (int i = 0; i < 4; i++) {
        __builtin_amdgcn_raw_ptr_buffer_load_lds(rk, sink, 16, pf + i * 16 * 2048, 0, 0, 0);
        __builtin_amdgcn_raw_ptr_buffer_load_lds(rv, sink, 16, pf + i * 16 * 2048, 0, 0, 0);
        __builtin_amdgcn_raw_ptr_buffer_load_lds(ru, sink, 16, pf + i * 16 * 2048, 0, 0, 0);
      }
    }
    float dt = rb(expf(bf(pdt)));
    f32x2 zr, zi; cplx C;
    zr[0] = -rb(expf(blo(pAr))) * dt; zr[1] = -rb(expf(bhi(pAr))) * dt;
    zi[0] = blo(pAi) * dt; zi[1] = bhi(pAi) * dt;
    C.r[0] = 2.f * blo(pCr) * dt; C.r[1] = 2.f * bhi(pCr) * dt;
    C.i[0] = -2.f * blo(pCi) * dt; C.i[1] = -2.f * bhi(pCi) * dt;
    zr *= 1.44269504088896341f; zi *= 0.15915494309189535f;
    lptr tb = smem + OFF_T + w * TB_STRIDE;
    lptr tw = tb + lane * 8, tr = tb + j16 * TB_ROW + q * 16;
    cplx w1 = cexp_p(zr, zi, 1.f), w16 = cexp_p(zr, zi, 16.f), w256 = cexp_p(zr, zi, 256.f);
    u32x4 G[8];
    {
      cplx g; g.r = f32x2{1.f, 1.f}; g.i = f32x2{0.f, 0.f};
      tput(tw, g); g = w16; tput(tw + TB_ROW, g);
#pragma unroll
      for (int i = 2; i < 16; i++) { g = cmul(g, w16); tput(tw + i * TB_ROW, g); }
      wsync();
#pragma unroll
      for (int s = 0; s < 8; s++) G[s] = lds_r128(tr + s * 64);
      wsync();
    }
    w1 = conj(w1); w256 = conj(w256);
    cplx w2 = csq(w1), w4 = csq(w2), w8 = csq(w4), w512 = csq(w256), w1024 = csq(w512);
    lptr kd = kb + 32;
    if (lane < 8) lds_w32(kb + lane * 4, 0);
    {
      cplx e = C; tput(tw, e);
#pragma unroll
      for (int i = 1; i < 16; i++) { e = cmul(e, w1); tput(tw + i * TB_ROW, e); }
      f32x4 a = {0, 0, 0, 0};
      wsync();
#pragma unroll
      for (int s = 0; s < 8; s++) a = mfma(lds_r128(tr + s * 64), G[s], a);
      wsync();
      lds_w64(kd + (16 * j16 + 4 * q) * 2, u32x2{cvtpk(a[0], a[1]), cvtpk(a[2], a[3])});
    }
    {
      cplx e = cmul(C, w256); tput(tw, e);
#pragma unroll
      for (int i = 1; i < 8; i++) { e = cmul(e, w2); tput(tw + i * TB_ROW, e); }
      e = cmul(C, w512);
#pragma unroll
      for (int i = 0; i < 4; i++) { tput(tw + (8 + i) * TB_ROW, e); tput(tw + (12 + i) * TB_ROW, cmul(e, w256)); if (i < 3) e = cmul(e, w4); }
      f32x4 a = {0, 0, 0, 0};
      wsync();
#pragma unroll
      for (int s = 0; s < 8; s++) a = mfma(lds_r128(tr + s * 64), G[s], a);
      wsync();
      u32 p01 = cvtpk(a[0], a[1]), p23 = cvtpk(a[2], a[3]);
      u32 v00 = perm(p01, p01, 0x01000100), v11 = perm(p01, p01, 0x03020302), v22 = perm(p23, p23, 0x01000100), v33 = perm(p23, p23, 0x03020302);
      const int base = q < 2 ? 256 + 16 * j16 + 8 * q : 512 + 256 * (q - 2) + 16 * j16;
      lds_w128(kd + base * 2, q < 2 ? u32x4{v00, perm(p23, p01, 0x05040302), v22, v33} : u32x4{v00, p01, v11, v22});
      if (q >= 2) lds_w128(kd + base * 2 + 16, u32x4{v22, p23, v33, v33});
      wsync();
      if (q >= 2 && base > 512) lds_w32(kd + (base - 2) * 2, v00); else if (base > 256) lds_w16(kd + (base - 1) * 2, (u16)p01);
      wsync();
    }
    {
      cplx e0 = cmul(C, w1024), e1 = cmul(e0, w8);
#pragma unroll
      for (int i = 0; i < 4; i++) { tput(tw + (2 * i) * TB_ROW, e0); tput(tw + (2 * i + 1) * TB_ROW, e1); e0 = cmul(e0, w256); if (i < 3) e1 = cmul(e1, w256); }
      tput(tw + 8 * TB_ROW, e0);
      f32x4 a = {0, 0, 0, 0};
      wsync();
#pragma unroll
      for (int s = 0; s < 8; s++) a = mfma(lds_r128(tr + s * 64), G[s], a);
      wsync();
      u32 p01 = cvtpk(a[0], a[1]), p23 = cvtpk(a[2], a[3]);
      if (q < 2) {
#pragma unroll
        for (int hh = 0; hh < 2; hh++) {
          u32 p = hh ? p23 : p01;
          u32 v00 = perm(p, p, 0x01000100), v11 = perm(p, p, 0x03020302);
          int base = 1024 + 512 * q + 256 * hh + 16 * j16;
          lds_w128(kd + base * 2, u32x4{v00, v00, p, v11});
          lds_w128(kd + base * 2 + 16, u32x4{v11, v11, v11, v11});
        }
      }
      wsync();
      if (q < 2) {
#pragma unroll
        for (int hh = 0; hh < 2; hh++) {
          u32 p = hh ? p23 : p01;
          u32 v00 = perm(p, p, 0x01000100);
          int base = 1024 + 512 * q + 256 * hh + 16 * j16;
          if (base > 1024) lds_w64(kd + (base - 4) * 2, u32x2{v00, v00}); else lds_w32(kd + (base - 2) * 2, v00);
        }
      } else if (lane == 32) {
        u32 v00 = perm(p01, p01, 0x01000100);
        lds_w64(kd + 2044 * 2, u32x2{v00, v00});
      }
      wsync();
    }
    loadA(A);
    STAMP(1);
    segment<0>(A, y, xa, yw, rot, lane == 0, cnt, 4, lane, [&]() {});
    STAMP(2);
    segment<1>(A, y, xa, yw, rot, lane == 0, cnt, 4, lane, [&]() {});
    STAMP(3);
    segment<2>(A, y, xa, yw, rot, lane == 0, cnt, 4, lane, [&]() {});
    STAMP(4);
    segment<3>(A, y, xa, yw, rot, lane == 0, cnt, 4, lane, [&]() {});
    STAMP(5);
  } else {
    const rsrc_t rs = mkrsrc(ssmk, L * D * 2), rw = mkrsrc(gatew, D * 2);
    const int tb = tid - 256, wb = w - 4;
    const u32 epoch = __hip_atomic_load(gepoch, __ATOMIC_RELAXED, __HIP_MEMORY_SCOPE_AGENT);
    const int grow = b * 8 + wb * 2;
    u32x4 gx[4], gw[2], kk[4];
#pragma unroll
    for (int e = 0; e < 4; e++) gx[e] = bl128(ru, grow * 2048 + (e * 64 + lane) * 16);
#pragma unroll
    for (int e = 0; e < 2; e++) gw[e] = bl128(rw, (e * 64 + lane) * 16);
#pragma unroll
    for (int e = 0; e < 4; e++) kk[e] = bl128(rs, ch * 4096 + (e * 64 + lane) * 16);
    const u32x4 shw0 = ((const u32x4*)shk)[cg * 2], shw1 = ((const u32x4*)shk)[cg * 2 + 1];
    const u32x2 dsk = ((const u32x2*)Dskip)[cg], dss = ((const u32x2*)ssmD)[cg];
    asm volatile("s_waitcnt lgkmcnt(0)\n\ts_barrier" ::: "memory");
    const u32 xoff = (2 * tb * 256 + cg) * 8;
    u32x2 xu[4][2], xkv[4][2];
    XIn in[4];
    auto load_x = [&](int s) {
      const u32 o = xoff + s * 512 * 2048;
      const int hrow = 512 * s + 128 * wb - 3 + lane;
      in[s].k[0] = bl64(rk, o); in[s].k[1] = bl64(rk, o + 2048);
      in[s].h = bl64(rk, (lane < 3 && hrow >= 0) ? (u32)(hrow * 256 + cg) * 8 : OOB);
      in[s].v[0] = bl64(rv, o); in[s].v[1] = bl64(rv, o + 2048);
      xu[s][0] = bl64(ru, o); xu[s][1] = bl64(ru, o + 2048);
    };
    load_x(0);
    load_x(1);
    for (int i = tb; i < 32 * 32; i += 256) lds_w32(smem + OFF_X + (i >> 7) * XB_STRIDE + ((i >> 5) & 3) * XP_STRIDE + (i & 31) * 4, 0);
    if (lane < 8) lds_w32(kb + lane * 4, 0);
    const u32 tag = epoch + 1;
    {
      u32 gb[2];
#pragma unroll
      for (int rr = 0; rr < 2; rr++) {
        float s = 0.f;
#pragma unroll
        for (int h = 0; h < 2; h++) {
          u32x4 x = gx[rr * 2 + h], ww = gw[h];
#pragma unroll
          for (int e = 0; e < 4; e++) { u32 p = cvtpk(blo(x[e]) * blo(ww[e]), bhi(x[e]) * bhi(ww[e])); s += blo(p) + bhi(p); }
        }
#pragma unroll
        for (int o = 32; o >= 1; o >>= 1) s += __shfl_xor(s, o);
        float z = rb(s), g = 1.f / (1.f + expf(-z));
        gb[rr] = cvtpk(g, g) & 0xffffu;
      }
      if (lane == 0) __hip_atomic_store(gq + (grow >> 1), ((u64)tag << 32) | (gb[0] | gb[1] << 16), __ATOMIC_RELAXED, __HIP_MEMORY_SCOPE_AGENT);
    }
#pragma unroll
    for (int e = 0; e < 4; e++) lds_w128(kb + 32 + (e * 64 + lane) * 16, kk[e]);
    const bool odd2 = lane & 2;
    const u32 sel0 = odd2 ? 0x01000504u : 0x05040100u, sel1 = odd2 ? 0x03020706u : 0x07060302u;
    lptr xw = smem + OFF_X + (odd2 ? 2 * XB_STRIDE : 0) + (lane & 1) * 2 * XP_STRIDE + 128 + (tb >> 2) * 4;
    auto proc = [&](int s) {
      process_x(xw + s * 256, sel0, sel1, odd2, xu[s], in[s], shw0, shw1, xkv[s]);
      cnt_signal(cnt + 4 * s, lane);
    };
    proc(0);
    load_x(2);
    wsync();
    loadA(A);
    STAMP(1);
    lptr yr = smem + OFF_K + 32 + tb * 4;
    const u64* gp = gq + tb;
    u64 gr[4];
    auto epi = [&](int s) {
      cnt_wait(cnt + 16 + 4 * s, 8);
      epilogue(yr + s * 1024, xoff + s * 512 * 2048, tag, gp + s * 256, gr[s], xu[s], xkv[s], dsk, dss, rout);
    };
    segment<0>(A, y, xa, yw, rot, lane == 0, cnt, 4, lane, [&]() { proc(1); load_x(3); });
    STAMP(2);
    gr[0] = __hip_atomic_load(gp, __ATOMIC_RELAXED, __HIP_MEMORY_SCOPE_AGENT);
    segment<1>(A, y, xa, yw, rot, lane == 0, cnt, 4, lane, [&]() { proc(2); });
    STAMP(3);
    gr[1] = __hip_atomic_load(gp + 256, __ATOMIC_RELAXED, __HIP_MEMORY_SCOPE_AGENT);
    epi(0);
    STAMP(4);
    segment<2>(A, y, xa, yw, rot, lane == 0, cnt, 4, lane, [&]() { proc(3); });
    STAMP(5);
    gr[2] = __hip_atomic_load(gp + 512, __ATOMIC_RELAXED, __HIP_MEMORY_SCOPE_AGENT);
    gr[3] = __hip_atomic_load(gp + 768, __ATOMIC_RELAXED, __HIP_MEMORY_SCOPE_AGENT);
    epi(1);
    STAMP(6);
    segment<3>(A, y, xa, yw, rot, lane == 0, cnt, 4, lane, [&]() {});
    STAMP(7);
    epi(2);
    STAMP(8);
    epi(3);
    if (b == 0 && lane == 0) {
      u32 n = __hip_atomic_fetch_add((LDS u32*)(cnt + 32), 1u, __ATOMIC_RELAXED, __HIP_MEMORY_SCOPE_WORKGROUP);
      if (n == 3) __hip_atomic_store(gepoch, tag, __ATOMIC_RELAXED, __HIP_MEMORY_SCOPE_AGENT);
    }
    STAMP(9);
  }
}

extern "C" void kernel_launch(void* const* d_in, const int* in_sizes, int n_in, void* d_out, int out_size, void* d_ws,
                              size_t ws_size, hipStream_t stream) {
  (void)hipFuncSetAttribute((const void*)conv_kernel, hipFuncAttributeMaxDynamicSharedMemorySize, LDS_BYTES);
  conv_kernel<<<256, 512, LDS_BYTES, stream>>>(d_in[0], d_in[1], d_in[2], d_in[3], d_in[4], d_in[5], d_in[6], d_in[8], d_in[9],
                                               d_in[10], d_in[11], d_in[12], d_in[13], d_ws, d_out);
}
```

```cpp
#include <hip/hip_runtime.h>
#include <hip/hip_bf16.h>
#include <type_traits>

typedef unsigned int u32;
typedef unsigned short u16;
typedef unsigned long long u64;
typedef u32 u32x2 __attribute__((ext_vector_type(2)));
typedef u32 u32x4 __attribute__((ext_vector_type(4)));
typedef float f32x2 __attribute__((ext_vector_type(2)));
typedef float f32x4 __attribute__((ext_vector_type(4)));
typedef __bf16 bf16x2 __attribute__((ext_vector_type(2)));
typedef __bf16 bf16x8 __attribute__((ext_vector_type(8)));
typedef short s16x4 __attribute__((ext_vector_type(4)));
#define DI __device__ __forceinline__
#define LDS __attribute__((address_space(3)))

constexpr int L = 2048, D = 1024, NM = 128;
constexpr int KB_STRIDE = 4128;
constexpr int XP_STRIDE = 1184;
constexpr int XB_STRIDE = 4 * XP_STRIDE + 64;
constexpr int TB_ROW = 528;
constexpr int TB_STRIDE = 16 * TB_ROW;
constexpr int OFF_K = 0;
constexpr int OFF_X = OFF_K + 8 * KB_STRIDE;
constexpr int OFF_T = OFF_X + 8 * XB_STRIDE;
constexpr int OFF_MISC = OFF_T + 4 * TB_STRIDE;
constexpr int OFF_PF = OFF_MISC + 64;
constexpr int LDS_BYTES = OFF_PF + 4096;
#define GATE_EARLY 0
#define PREFETCH 1

DI u32 cvtpk(float lo, float hi) { f32x2 v = {lo, hi}; bf16x2 b = __builtin_convertvector(v, bf16x2); return __builtin_bit_cast(u32, b); }
DI float blo(u32 x) { return __uint_as_float(x << 16); }
DI float bhi(u32 x) { return __uint_as_float(x & 0xffff0000u); }
DI float rb(float x) { return blo(cvtpk(x, x)); }
DI float bf(u16 x) { return __uint_as_float((u32)x << 16); }
DI f32x4 mfma(u32x4 a, u32x4 b, f32x4 c) {
  return __builtin_amdgcn_mfma_f32_16x16x32_bf16(__builtin_bit_cast(bf16x8, a), __builtin_bit_cast(bf16x8, b), c, 0, 0, 0);
}
typedef LDS char* lptr;
DI u32x2 lds_r64(lptr p) { return *(LDS u32x2*)p; }
DI u32x4 lds_r128(lptr p) { return *(LDS u32x4*)p; }
DI void lds_w32(lptr p, u32 v) { *(LDS u32*)p = v; }
DI void lds_w16(lptr p, u16 v) { *(LDS u16*)p = v; }
DI void lds_w64(lptr p, u32x2 v) { *(LDS u32x2*)p = v; }
DI void lds_w128(lptr p, u32x4 v) { *(LDS u32x4*)p = v; }
DI u32x2 lds_tr(lptr p) {
  typedef short v4i16_t __attribute__((ext_vector_type(4)));
  return __builtin_bit_cast(u32x2, __builtin_amdgcn_ds_read_tr16_b64_v4i16((LDS v4i16_t*)p));
}
DI void wsync() { __builtin_amdgcn_fence(__ATOMIC_ACQ_REL, "wavefront"); __builtin_amdgcn_wave_barrier(); }
DI u32 perm(u32 hi, u32 lo, u32 sel) { return __builtin_amdgcn_perm(hi, lo, sel); }

#define STAMP(i)
struct cplx { f32x2 r, i; };
DI cplx cmul(cplx a, cplx b) { cplx c; c.r = a.r * b.r - a.i * b.i; c.i = a.r * b.i + a.i * b.r; return c; }
DI cplx csq(cplx a) { cplx c; c.r = a.r * a.r - a.i * a.i; f32x2 t = a.r * a.i; c.i = t + t; return c; }
DI cplx conj(cplx a) { cplx c; c.r = a.r; c.i = -a.i; return c; }
DI cplx cexp_p(f32x2 zr2, f32x2 zi_rev, float p) {
  cplx c;
#pragma unroll
  for (int m = 0; m < 2; m++) {
    float mag = __builtin_amdgcn_exp2f(zr2[m] * p);
    float ph = __builtin_amdgcn_fractf(zi_rev[m] * p);
    c.r[m] = mag * __builtin_amdgcn_cosf(ph);
    c.i[m] = mag * __builtin_amdgcn_sinf(ph);
  }
  return c;
}
DI void tput(lptr trow, cplx e) { u32x2 v; v[0] = cvtpk(e.r[0], e.i[0]); v[1] = cvtpk(e.r[1], e.i[1]); lds_w64(trow, v); }

typedef __amdgpu_buffer_rsrc_t rsrc_t;
DI rsrc_t mkrsrc(const void* p, u32 bytes) { return __builtin_amdgcn_make_buffer_rsrc((void*)p, 0, bytes, 0x00020000); }
DI u32 bl16(rsrc_t r, u32 off) { return __builtin_amdgcn_raw_buffer_load_b16(r, off, 0, 0); }
DI u32 bl32(rsrc_t r, u32 off) { return __builtin_amdgcn_raw_buffer_load_b32(r, off, 0, 0); }
DI u32x2 bl64(rsrc_t r, u32 off) { return __builtin_bit_cast(u32x2, __builtin_amdgcn_raw_buffer_load_b64(r, off, 0, 0)); }
DI u32x4 bl128(rsrc_t r, u32 off) { return __builtin_bit_cast(u32x4, __builtin_amdgcn_raw_buffer_load_b128(r, off, 0, 0)); }
constexpr u32 OOB = 0x80000000u;

template <int OFF> DI void rdB(u32x2& lo, u32x2& hi, u32 a) {
  asm volatile("ds_read_b64 %0, %2 offset:%3\n\tds_read_b64 %1, %2 offset:%4" : "=&v"(lo), "=&v"(hi) : "v"(a), "i"(OFF), "i"(OFF + 8) : "memory");
}
struct BF { u32x2 lo[4], hi[4]; };
template <int SG> DI void loadB(BF& b, u32 xa) {
  rdB<0 * XP_STRIDE + SG * 64>(b.lo[0], b.hi[0], xa);
  rdB<1 * XP_STRIDE + SG * 64>(b.lo[1], b.hi[1], xa);
  rdB<2 * XP_STRIDE + SG * 64>(b.lo[2], b.hi[2], xa);
  rdB<3 * XP_STRIDE + SG * 64>(b.lo[3], b.hi[3], xa);
}
DI void waitB(BF& b) {
  asm volatile("s_waitcnt lgkmcnt(0)" : "+v"(b.lo[0]), "+v"(b.hi[0]), "+v"(b.lo[1]), "+v"(b.hi[1]), "+v"(b.lo[2]), "+v"(b.hi[2]), "+v"(b.lo[3]), "+v"(b.hi[3]) :: "memory");
}
typedef f32x4 Acc[2][4][2];
template <int SG, int P0, int P1> DI void groupMM(const u32x4 (&A)[16], const BF& b, Acc& acc) {
#pragma unroll
  for (int r = 0; r < 4; r++) {
    u32x4 B = {b.lo[r][0], b.lo[r][1], b.hi[r][0], b.hi[r][1]};
#pragma unroll
    for (int sl = 0; sl < 2; sl++) {
      const int P = sl ? P1 : P0;
      if (P < 0) continue;
#pragma unroll
      for (int hh = 0; hh < 2; hh++) {
        const int c = 2 * (2 * P + hh) - (SG - 1);
        if (c >= 0) acc[sl][r][hh] = mfma(A[c], B, SG == 0 ? f32x4{0, 0, 0, 0} : acc[sl][r][hh]);
      }
    }
  }
}
template <int S, int P0, int P1, bool PRE, int NEXT> DI void block(const u32x4 (&A)[16], Acc& acc, BF& b0, u32 xa) {
  BF b1;
  if (!PRE) { loadB<4 * S>(b0, xa); waitB(b0); }
  loadB<4 * S + 1>(b1, xa);
  __builtin_amdgcn_sched_barrier(0);
  groupMM<4 * S, P0, P1>(A, b0, acc);
  __builtin_amdgcn_sched_barrier(0);
  waitB(b1);
  loadB<4 * S + 2>(b0, xa);
  __builtin_amdgcn_sched_barrier(0);
  groupMM<4 * S + 1, P0, P1>(A, b1, acc);
  __builtin_amdgcn_sched_barrier(0);
  waitB(b0);
  loadB<4 * S + 3>(b1, xa);
  __builtin_amdgcn_sched_barrier(0);
  groupMM<4 * S + 2, P0, P1>(A, b0, acc);
  __builtin_amdgcn_sched_barrier(0);
  waitB(b1);
  if (NEXT >= 0) loadB<(NEXT >= 0 ? NEXT : 0)>(b0, xa);
  __builtin_amdgcn_sched_barrier(0);
  groupMM<4 * S + 3, P0, P1>(A, b1, acc);
  __builtin_amdgcn_sched_barrier(0);
  if (NEXT >= 0) waitB(b0);
}
template <int P> DI void finish(const f32x4 (&a)[4][2], float (&carry)[3], lptr yw, int rot, bool l0) {
  float t[2][3];
#pragma unroll
  for (int hh = 0; hh < 2; hh++) {
    float c0 = a[1][hh][3] + a[2][hh][2] + a[3][hh][1], c1 = a[2][hh][3] + a[3][hh][2], c2 = a[3][hh][3];
    t[hh][0] = __int_as_float(__builtin_amdgcn_ds_bpermute(rot, __float_as_int(c0)));
    t[hh][1] = __int_as_float(__builtin_amdgcn_ds_bpermute(rot, __float_as_int(c1)));
    t[hh][2] = __int_as_float(__builtin_amdgcn_ds_bpermute(rot, __float_as_int(c2)));
  }
#pragma unroll
  for (int hh = 0; hh < 2; hh++) {
    float x[3];
#pragma unroll
    for (int k = 0; k < 3; k++) x[k] = l0 ? (hh ? t[0][k] : (P ? carry[k] : 0.f)) : t[hh][k];
    float y0 = a[0][hh][0] + x[0], y1 = a[0][hh][1] + a[1][hh][0] + x[1];
    float y2 = a[0][hh][2] + a[1][hh][1] + a[2][hh][0] + x[2], y3 = a[0][hh][3] + a[1][hh][2] + a[2][hh][1] + a[3][hh][0];
    lds_w64(yw + 512 * (2 * P + hh), u32x2{cvtpk(y0, y1), cvtpk(y2, y3)});
  }
#pragma unroll
  for (int k = 0; k < 3; k++) carry[k] = t[1][k];
}

DI void cnt_signal(lptr p, int lane) {
  asm volatile("s_waitcnt lgkmcnt(0)" ::: "memory");
  if (lane == 0) __hip_atomic_fetch_add((LDS u32*)p, 1u, __ATOMIC_RELAXED, __HIP_MEMORY_SCOPE_WORKGROUP);
  asm volatile("" ::: "memory");
}
DI void cnt_wait(lptr p, u32 target) {
  asm volatile("" ::: "memory");
#pragma unroll 1
  for (int it = 0; it < (1 << 22); it++) {
    u32 v = __builtin_amdgcn_readfirstlane(__hip_atomic_load((LDS u32*)p, __ATOMIC_RELAXED, __HIP_MEMORY_SCOPE_WORKGROUP));
    if (v >= target) break;
    __builtin_amdgcn_s_sleep(1);
  }
  asm volatile("" ::: "memory");
}

struct XIn { u32x2 k[2], v[2], h; };
DI void process_x(lptr xw, u32 sel0, u32 sel1, bool odd2, const u32x2 (&xu)[2], const XIn& in, u32x4 shw0, u32x4 shw1,
                  u32x2 (&xkv)[2]) {
  u32x2 kr[5];
#pragma unroll
  for (int d = 0; d < 2; d++) {
    u32 h1 = __builtin_amdgcn_update_dpp(0u, in.h[d], 0x101, 0xf, 0xf, true);
    u32 h2 = __builtin_amdgcn_update_dpp(0u, in.h[d], 0x102, 0xf, 0xf, true);
    kr[1][d] = __builtin_amdgcn_update_dpp(h1, in.k[0][d], 0x138, 0xf, 0xf, false);
    kr[2][d] = __builtin_amdgcn_update_dpp(h2, in.k[1][d], 0x138, 0xf, 0xf, false);
    kr[0][d] = __builtin_amdgcn_update_dpp(in.h[d], kr[2][d], 0x138, 0xf, 0xf, false);
    kr[3][d] = in.k[0][d]; kr[4][d] = in.k[1][d];
  }
#pragma unroll
  for (int cp = 0; cp < 2; cp++) {
    u32x4 sw = cp ? shw1 : shw0;
    f32x2 t0 = {blo(sw[0]), blo(sw[2])}, t1 = {bhi(sw[0]), bhi(sw[2])}, t2 = {blo(sw[1]), blo(sw[3])}, t3 = {bhi(sw[1]), bhi(sw[3])};
    f32x2 kf[5];
#pragma unroll
    for (int e = 0; e < 5; e++) kf[e] = f32x2{blo(kr[e][cp]), bhi(kr[e][cp])};
#pragma unroll
    for (int e = 0; e < 2; e++) {
      f32x2 p3 = kf[e] * t3, p2 = kf[e + 1] * t2, p1 = kf[e + 2] * t1, p0 = kf[e + 3] * t0;
      u32 u3 = cvtpk(p3[0], p3[1]), u2 = cvtpk(p2[0], p2[1]), u1 = cvtpk(p1[0], p1[1]), u0 = cvtpk(p0[0], p0[1]);
      f32x2 s = f32x2{blo(u3), bhi(u3)} + f32x2{blo(u2), bhi(u2)};
      u32 us = cvtpk(s[0], s[1]);
      s = f32x2{blo(us), bhi(us)} + f32x2{blo(u1), bhi(u1)}; us = cvtpk(s[0], s[1]);
      s = f32x2{blo(us), bhi(us)} + f32x2{blo(u0), bhi(u0)}; us = cvtpk(s[0], s[1]);
      s = f32x2{blo(us), bhi(us)} * f32x2{blo(in.v[e][cp]), bhi(in.v[e][cp])};
      xkv[e][cp] = cvtpk(s[0], s[1]);
    }
  }
#pragma unroll
  for (int arr = 0; arr < 2; arr++) {
#pragma unroll
    for (int a = 0; a < 2; a++) {
      u32x2 x = arr ? xkv[a] : xu[a];
      u32 keep = odd2 ? x[1] : x[0], send = odd2 ? x[0] : x[1];
      u32 other = __builtin_amdgcn_update_dpp(0u, send, 0x4e, 0xf, 0xf, true);
      lds_w32(xw + arr * 4 * XB_STRIDE + a * XP_STRIDE, perm(other, keep, sel0));
      lds_w32(xw + (arr * 4 + 1) * XB_STRIDE + a * XP_STRIDE, perm(other, keep, sel1));
    }
  }
}

DI void epilogue(lptr yr, u32 ooff, u32 tag, const u64* gp, u64 gr, const u32x2 (&xu)[2], const u32x2 (&xkv)[2], u32x2 dsk,
                 u32x2 dss, rsrc_t rout) {
  u32 ya[4], yb[4];
#pragma unroll
  for (int c = 0; c < 4; c++) {
    ya[c] = *(LDS u32*)(yr + c * KB_STRIDE);
    yb[c] = *(LDS u32*)(yr + (4 + c) * KB_STRIDE);
  }
  if (!__all((u32)(gr >> 32) == tag)) {
#pragma unroll 1
    for (int it = 0; it < (1 << 16); it++) {
      __builtin_amdgcn_s_sleep(2);
      gr = __hip_atomic_load(gp, __ATOMIC_RELAXED, __HIP_MEMORY_SCOPE_AGENT);
      if (__all((u32)(gr >> 32) == tag)) break;
    }
  }
#pragma unroll
  for (int e = 0; e < 2; e++) {
    float g = e ? bhi((u32)gr) : blo((u32)gr), g1 = rb(1.f - g);
    u32x2 o;
#pragma unroll
    for (int cp = 0; cp < 2; cp++) {
      float res[2];
#pragma unroll
      for (int hl = 0; hl < 2; hl++) {
        int c = cp * 2 + hl;
        float uu = hl ? bhi(xu[e][cp]) : blo(xu[e][cp]);
        float kv = hl ? bhi(xkv[e][cp]) : blo(xkv[e][cp]);
        float d1 = hl ? bhi(dsk[cp]) : blo(dsk[cp]);
        float d2 = hl ? bhi(dss[cp]) : blo(dss[cp]);
        float y1 = e ? bhi(ya[c]) : blo(ya[c]);
        float y2 = e ? bhi(yb[c]) : blo(yb[c]);
        float a = rb(y1 + rb(uu * d1));
        float bb = rb(uu * rb(y2 + rb(kv * d2)));
        res[hl] = rb(g * a) + rb(g1 * bb);
      }
      o[cp] = cvtpk(res[0], res[1]);
    }
    __builtin_amdgcn_raw_buffer_store_b64(__builtin_bit_cast(__attribute__((ext_vector_type(2))) unsigned, o), rout, ooff + e * 2048, 0, 0);
  }
}

template <class F> DI void conv(const u32x4 (&A)[16], u32 xa, lptr yw, int rot, int lane, lptr cnt, F hook) {
  Acc acc; BF b0; float carry[3];
  const bool l0 = lane == 0;
  cnt_wait(cnt + 0, 4);
  block<0, 0, 1, false, 0>(A, acc, b0, xa);
  finish<0>(acc[0], carry, yw, rot, l0);
  cnt_signal(cnt + 16, lane);
  hook(std::integral_constant<int, 0>{});
  block<0, 2, -1, true, -1>(A, acc, b0, xa);
  hook(std::integral_constant<int, 1>{});
  cnt_wait(cnt + 4, 4);
  block<1, 2, 1, false, 0>(A, acc, b0, xa);
  finish<1>(acc[1], carry, yw, rot, l0);
  cnt_signal(cnt + 20, lane);
  hook(std::integral_constant<int, 2>{});
  block<0, -1, 3, true, 4>(A, acc, b0, xa);
  hook(std::integral_constant<int, 3>{});
  block<1, -1, 3, true, -1>(A, acc, b0, xa);
  hook(std::integral_constant<int, 4>{});
  cnt_wait(cnt + 8, 4);
  block<2, 2, 3, false, -1>(A, acc, b0, xa);
  finish<2>(acc[0], carry, yw, rot, l0);
  cnt_signal(cnt + 24, lane);
  hook(std::integral_constant<int, 5>{});
  cnt_wait(cnt + 12, 4);
  block<3, -1, 3, false, -1>(A, acc, b0, xa);
  finish<3>(acc[1], carry, yw, rot, l0);
  cnt_signal(cnt + 28, lane);
}

__global__ __launch_bounds__(512, 2) void conv_kernel(
    const void* __restrict__ gu, const void* __restrict__ logAr, const void* __restrict__ Aim, const void* __restrict__ Cre,
    const void* __restrict__ Cim, const void* __restrict__ logdt, const void* __restrict__ Dskip, const void* __restrict__ gk,
    const void* __restrict__ gv, const void* __restrict__ shk, const void* __restrict__ ssmk, const void* __restrict__ ssmD,
    const void* __restrict__ gatew, void* ws, void* __restrict__ out) {
  extern __shared__ __attribute__((aligned(16))) char smem_[];
  lptr smem = (lptr)smem_;
  const int b = blockIdx.x, cg = (b & 7) * 32 + (b >> 3);
  const int tid = threadIdx.x, lane = tid & 63, w = __builtin_amdgcn_readfirstlane(tid >> 6);
  const int ch = cg * 4 + (w & 3);
  const int j16 = lane & 15, q = lane >> 4;
  const bool isA = w < 4;
  lptr kb = smem + OFF_K + w * KB_STRIDE;
  u64* gq = (u64*)ws;
  u32* gepoch = (u32*)((char*)ws + 65536);
  const rsrc_t ru = mkrsrc(gu, L * D * 2), rk = mkrsrc(gk, L * D * 2), rv = mkrsrc(gv, L * D * 2), rout = mkrsrc(out, L * D * 2);
  STAMP(0);
  lptr cnt = smem + OFF_MISC;
  if (tid < 16) lds_w32(cnt + tid * 4, 0);
  asm volatile("s_waitcnt lgkmcnt(0)\n\ts_barrier" ::: "memory");
  auto loadA = [&](u32x4 (&A)[16]) {
    const int m = lane & 3, ep = (lane >> 2) & 3;
    lptr ka = kb + 32 + (-12 + 4 * m + 4 * (8 * q + 7 - ep)) * 2;
#pragma unroll
    for (int c = 0; c < 16; c++) {
      u32x2 lo = lds_tr(ka + c * 256), hi = lds_tr(ka + c * 256 - 32);
      A[c] = u32x4{lo[0], lo[1], hi[0], hi[1]};
    }
  };
  const u32 xa = (u32)(size_t)(smem + OFF_X + w * XB_STRIDE + (64 - 32 + 4 * j16 - 8 * q - 4) * 2);
  const int rot = (lane == 0 ? 63 : q == 0 ? lane + 47 : lane - 16) * 4;
  lptr yw = kb + 32 + (16 * j16 + 4 * q) * 2;
  u32x4 A[16];

  if (isA) {
    const rsrc_t r1 = mkrsrc(logAr, D * NM * 2), r2 = mkrsrc(Aim, D * NM * 2), r3 = mkrsrc(Cre, D * NM * 2), r4 = mkrsrc(Cim, D * NM * 2);
    const u32 po = (ch * 64 + lane) * 4;
    u32 pAr = bl32(r1, po), pAi = bl32(r2, po), pCr = bl32(r3, po), pCi = bl32(r4, po);
    u32 pdt = bl16(mkrsrc(logdt, D * 2), ch * 2);
    {
      const u32 pf = ((b >> 3) * 64 + (tid >> 4)) * 2048 + (b & 7) * 256 + (tid & 15) * 16;
      LDS void* sink = (LDS void*)(smem + OFF_PF + w * 1024);
#pragma unroll
      for (int i = 0; i < 4; i++) {
        __builtin_amdgcn_raw_ptr_buffer_load_lds(rk, sink, 16, pf + i * 16 * 2048, 0, 0, 0);
        __builtin_amdgcn_raw_ptr_buffer_load_lds(rv, sink, 16, pf + i * 16 * 2048, 0, 0, 0);
        __builtin_amdgcn_raw_ptr_buffer_load_lds(ru, sink, 16, pf + i * 16 * 2048, 0, 0, 0);
      }
    }
    STAMP(10);
    float dt = rb(expf(bf(pdt)));
    f32x2 zr, zi; cplx C;
    zr[0] = -rb(expf(blo(pAr))) * dt; zr[1] = -rb(expf(bhi(pAr))) * dt;
    zi[0] = blo(pAi) * dt; zi[1] = bhi(pAi) * dt;
    C.r[0] = 2.f * blo(pCr) * dt; C.r[1] = 2.f * bhi(pCr) * dt;
    C.i[0] = -2.f * blo(pCi) * dt; C.i[1] = -2.f * bhi(pCi) * dt;
    zr *= 1.44269504088896341f; zi *= 0.15915494309189535f;
    STAMP(11);
    lptr tb = smem + OFF_T + w * TB_STRIDE;
    lptr tw = tb + lane * 8, tr = tb + j16 * TB_ROW + q * 16;
    cplx w1 = cexp_p(zr, zi, 1.f), w16 = cexp_p(zr, zi, 16.f), w256 = cexp_p(zr, zi, 256.f);
    u32x4 G[8];
    {
      cplx g; g.r = f32x2{1.f, 1.f}; g.i = f32x2{0.f, 0.f};
      tput(tw, g); g = w16; tput(tw + TB_ROW, g);
#pragma unroll
      for (int i = 2; i < 16; i++) { g = cmul(g, w16); tput(tw + i * TB_ROW, g); }
      wsync();
#pragma unroll
      for (int s = 0; s < 8; s++) G[s] = lds_r128(tr + s * 64);
      wsync();
    }
    STAMP(12);
    w1 = conj(w1); w256 = conj(w256);
    cplx w2 = csq(w1), w4 = csq(w2), w8 = csq(w4), w512 = csq(w256), w1024 = csq(w512);
    lptr kd = kb + 32;
    if (lane < 8) lds_w32(kb + lane * 4, 0);
    {
      cplx e = C; tput(tw, e);
#pragma unroll
      for (int i = 1; i < 16; i++) { e = cmul(e, w1); tput(tw + i * TB_ROW, e); }
      f32x4 a = {0, 0, 0, 0};
      wsync();
#pragma unroll
      for (int s = 0; s < 8; s++) a = mfma(lds_r128(tr + s * 64), G[s], a);
      wsync();
      lds_w64(kd + (16 * j16 + 4 * q) * 2, u32x2{cvtpk(a[0], a[1]), cvtpk(a[2], a[3])});
    }
    STAMP(13);
    {
      cplx e = cmul(C, w256); tput(tw, e);
#pragma unroll
      for (int i = 1; i < 8; i++) { e = cmul(e, w2); tput(tw + i * TB_ROW, e); }
      e = cmul(C, w512);
#pragma unroll
      for (int i = 0; i < 4; i++) { tput(tw + (8 + i) * TB_ROW, e); tput(tw + (12 + i) * TB_ROW, cmul(e, w256)); if (i < 3) e = cmul(e, w4); }
      f32x4 a = {0, 0, 0, 0};
      wsync();
#pragma unroll
      for (int s = 0; s < 8; s++) a = mfma(lds_r128(tr + s * 64), G[s], a);
      wsync();
      u32 p01 = cvtpk(a[0], a[1]), p23 = cvtpk(a[2], a[3]);
      u32 v00 = perm(p01, p01, 0x01000100), v11 = perm(p01, p01, 0x03020302), v22 = perm(p23, p23, 0x01000100), v33 = perm(p23, p23, 0x03020302);
      const int base = q < 2 ? 256 + 16 * j16 + 8 * q : 512 + 256 * (q - 2) + 16 * j16;
      lds_w128(kd + base * 2, q < 2 ? u32x4{v00, perm(p23, p01, 0x05040302), v22, v33} : u32x4{v00, p01, v11, v22});
      if (q >= 2) lds_w128(kd + base * 2 + 16, u32x4{v22, p23, v33, v33});
      wsync();
      if (q >= 2 && base > 512) lds_w32(kd + (base - 2) * 2, v00); else if (base > 256) lds_w16(kd + (base - 1) * 2, (u16)p01);
      wsync();
    }
    STAMP(14);
    {
      cplx e0 = cmul(C, w1024), e1 = cmul(e0, w8);
#pragma unroll
      for (int i = 0; i < 4; i++) { tput(tw + (2 * i) * TB_ROW, e0); tput(tw + (2 * i + 1) * TB_ROW, e1); e0 = cmul(e0, w256); if (i < 3) e1 = cmul(e1, w256); }
      tput(tw + 8 * TB_ROW, e0);
      f32x4 a = {0, 0, 0, 0};
      wsync();
#pragma unroll
      for (int s = 0; s < 8; s++) a = mfma(lds_r128(tr + s * 64), G[s], a);
      wsync();
      u32 p01 = cvtpk(a[0], a[1]), p23 = cvtpk(a[2], a[3]);
      if (q < 2) {
#pragma unroll
        for (int hh = 0; hh < 2; hh++) {
          u32 p = hh ? p23 : p01;
          u32 v00 = perm(p, p, 0x01000100), v11 = perm(p, p, 0x03020302);
          int base = 1024 + 512 * q + 256 * hh + 16 * j16;
          lds_w128(kd + base * 2, u32x4{v00, v00, p, v11});
          lds_w128(kd + base * 2 + 16, u32x4{v11, v11, v11, v11});
        }
      }
      wsync();
      if (q < 2) {
#pragma unroll
        for (int hh = 0; hh < 2; hh++) {
          u32 p = hh ? p23 : p01;
          u32 v00 = perm(p, p, 0x01000100);
          int base = 1024 + 512 * q + 256 * hh + 16 * j16;
          if (base > 1024) lds_w64(kd + (base - 4) * 2, u32x2{v00, v00}); else lds_w32(kd + (base - 2) * 2, v00);
        }
      } else if (lane == 32) {
        u32 v00 = perm(p01, p01, 0x01000100);
        lds_w64(kd + 2044 * 2, u32x2{v00, v00});
      }
      wsync();
    }
    STAMP(15);
    loadA(A);
    STAMP(1);
    conv(A, xa, yw, rot, lane, cnt, [&](auto k) { STAMP(2 + k.value); });
    STAMP(8);
  } else {
    const rsrc_t rs = mkrsrc(ssmk, L * D * 2), rw = mkrsrc(gatew, D * 2);
    const int tb = tid - 256, wb = w - 4;
    const u32 par = bl32(mkrsrc(shk, D * 8), lane < 8 ? cg * 32 + lane * 4 : OOB) | bl32(mkrsrc(Dskip, D * 2), (lane >> 1) == 4 ? cg * 8 + (lane & 1) * 4 : OOB) |
                    bl32(mkrsrc(ssmD, D * 2), (lane >> 1) == 5 ? cg * 8 + (lane & 1) * 4 : OOB);
    const u32 xoff = (2 * tb * 256 + cg) * 8;
    u32x2 xu[4][2], xkv[4][2];
    XIn in[4];
    auto load_x = [&](int s) {
      const u32 o = xoff + s * 512 * 2048;
      const int hrow = 512 * s + 128 * wb - 3 + lane;
      in[s].k[0] = bl64(rk, o); in[s].k[1] = bl64(rk, o + 2048);
      in[s].h = bl64(rk, (lane < 3 && hrow >= 0) ? (u32)(hrow * 256 + cg) * 8 : OOB);
      in[s].v[0] = bl64(rv, o); in[s].v[1] = bl64(rv, o + 2048);
      xu[s][0] = bl64(ru, o); xu[s][1] = bl64(ru, o + 2048);
      __builtin_amdgcn_sched_barrier(0);
    };
    auto landed = [&](int s) { asm volatile("" : "+v"(xu[s][1])); __builtin_amdgcn_sched_barrier(0); };
    load_x(0);
    const u32 epoch = __hip_atomic_load(gepoch, __ATOMIC_RELAXED, __HIP_MEMORY_SCOPE_AGENT);
    const int grow = b * 8 + wb * 2;
    u32x4 gx[4], gw[2], kk[4];
#pragma unroll
    for (int e = 0; e < 4; e++) kk[e] = bl128(rs, ch * 4096 + (e * 64 + lane) * 16);
#pragma unroll
    for (int e = 0; e < 4; e++) gx[e] = bl128(ru, grow * 2048 + (e * 64 + lane) * 16);
#pragma unroll
    for (int e = 0; e < 2; e++) gw[e] = bl128(rw, (e * 64 + lane) * 16);
    STAMP(10);
    for (int i = tb; i < 32 * 32; i += 256) lds_w32(smem + OFF_X + (i >> 7) * XB_STRIDE + ((i >> 5) & 3) * XP_STRIDE + (i & 31) * 4, 0);
    if (lane < 8) lds_w32(kb + lane * 4, 0);
    const bool odd2 = lane & 2;
    const u32 sel0 = odd2 ? 0x01000504u : 0x05040100u, sel1 = odd2 ? 0x03020706u : 0x07060302u;
    lptr xw = smem + OFF_X + (odd2 ? 2 * XB_STRIDE : 0) + (lane & 1) * 2 * XP_STRIDE + 128 + (tb >> 2) * 4;
    const u32 tag = epoch + 1;
    auto gate = [&]() {
      u32 gb[2];
#pragma unroll
      for (int rr = 0; rr < 2; rr++) {
        float s = 0.f;
#pragma unroll
        for (int h = 0; h < 2; h++) {
          u32x4 x = gx[rr * 2 + h], ww = gw[h];
#pragma unroll
          for (int e = 0; e < 4; e++) { u32 p = cvtpk(blo(x[e]) * blo(ww[e]), bhi(x[e]) * bhi(ww[e])); s += blo(p) + bhi(p); }
        }
#pragma unroll
        for (int o = 32; o >= 1; o >>= 1) s += __shfl_xor(s, o);
        float z = rb(s), g = 1.f / (1.f + expf(-z));
        gb[rr] = cvtpk(g, g) & 0xffffu;
      }
      if (lane == 0) __hip_atomic_store(gq + (grow >> 1), ((u64)tag << 32) | (gb[0] | gb[1] << 16), __ATOMIC_RELAXED, __HIP_MEMORY_SCOPE_AGENT);
    };
    STAMP(11);
    landed(0);
    u32x4 shw0, shw1; u32x2 dsk, dss;
#pragma unroll
    for (int e = 0; e < 4; e++) { shw0[e] = __builtin_amdgcn_readlane(par, e); shw1[e] = __builtin_amdgcn_readlane(par, 4 + e); }
#pragma unroll
    for (int e = 0; e < 2; e++) { dsk[e] = __builtin_amdgcn_readlane(par, 8 + e); dss[e] = __builtin_amdgcn_readlane(par, 10 + e); }
    STAMP(12);
    auto proc = [&](int s) {
      process_x(xw + s * 256, sel0, sel1, odd2, xu[s], in[s], shw0, shw1, xkv[s]);
      cnt_signal(cnt + 4 * s, lane);
    };
    load_x(1);
    STAMP(13);
    proc(0);
    STAMP(14);
#pragma unroll
    for (int e = 0; e < 4; e++) lds_w128(kb + 32 + (e * 64 + lane) * 16, kk[e]);
    STAMP(15);
    wsync();
    loadA(A);
    STAMP(1);
    lptr yr = smem + OFF_K + 32 + tb * 4;
    const u64* gp = gq + tb;
    u64 gr[4];
    auto ldg = [&](int s) { gr[s] = __hip_atomic_load(gp + s * 256, __ATOMIC_RELAXED, __HIP_MEMORY_SCOPE_AGENT); };
    auto epi = [&](int s) {
      cnt_wait(cnt + 16 + 4 * s, 8);
      epilogue(yr + s * 1024, xoff + s * 512 * 2048, tag, gp + s * 256, gr[s], xu[s], xkv[s], dsk, dss, rout);
    };
    conv(A, xa, yw, rot, lane, cnt, [&](auto k) {
      constexpr int K = k.value;
      STAMP(2 + K);
      if (K == 0) { landed(1); load_x(2); proc(1); }
      if (K == 1) gate();
      if (K == 1) load_x(3);
      if (K == 2) proc(2);
      if (K == 3) { proc(3); ldg(0); ldg(1); }
      if (K == 4) { epi(0); ldg(2); ldg(3); }
      if (K == 5) epi(1);
    });
    STAMP(8);
    epi(2);
    epi(3);
    if (b == 0 && lane == 0) {
      u32 n = __hip_atomic_fetch_add((LDS u32*)(cnt + 32), 1u, __ATOMIC_RELAXED, __HIP_MEMORY_SCOPE_WORKGROUP);
      if (n == 3) __hip_atomic_store(gepoch, tag, __ATOMIC_RELAXED, __HIP_MEMORY_SCOPE_AGENT);
    }
    STAMP(9);
  }
}

extern "C" void kernel_launch(void* const* d_in, const int* in_sizes, int n_in, void* d_out, int out_size, void* d_ws,
                              size_t ws_size, hipStream_t stream) {
  (void)hipFuncSetAttribute((const void*)conv_kernel, hipFuncAttributeMaxDynamicSharedMemorySize, LDS_BYTES);
  conv_kernel<<<256, 512, LDS_BYTES, stream>>>(d_in[0], d_in[1], d_in[2], d_in[3], d_in[4], d_in[5], d_in[6], d_in[8], d_in[9],
                                               d_in[10], d_in[11], d_in[12], d_in[13], d_ws, d_out);
}
```

```cpp
#include <hip/hip_runtime.h>
#include <hip/hip_bf16.h>
#include <type_traits>

typedef unsigned int u32;
typedef unsigned short u16;
typedef unsigned long long u64;
typedef u32 u32x2 __attribute__((ext_vector_type(2)));
typedef u32 u32x4 __attribute__((ext_vector_type(4)));
typedef float f32x2 __attribute__((ext_vector_type(2)));
typedef float f32x4 __attribute__((ext_vector_type(4)));
typedef __bf16 bf16x2 __attribute__((ext_vector_type(2)));
typedef __bf16 bf16x8 __attribute__((ext_vector_type(8)));
typedef short s16x4 __attribute__((ext_vector_type(4)));
#define DI __device__ __forceinline__
#define LDS __attribute__((address_space(3)))

constexpr int L = 2048, D = 1024, NM = 128;
constexpr int KB_STRIDE = 4128;
constexpr int XP_STRIDE = 1184;
constexpr int XB_STRIDE = 4 * XP_STRIDE + 64;
constexpr int TB_ROW = 528;
constexpr int TB_STRIDE = 16 * TB_ROW;
constexpr int OFF_K = 0;
constexpr int OFF_X = OFF_K + 8 * KB_STRIDE;
constexpr int OFF_T = OFF_X + 8 * XB_STRIDE;
constexpr int OFF_MISC = OFF_T + 4 * TB_STRIDE;
constexpr int OFF_PF = OFF_MISC + 64;
constexpr int LDS_BYTES = OFF_PF + 4096 + 256;
constexpr int CODE_BYTES = 32768;
#define GATE_EARLY 0
#define CODEPF 1
#define PREFETCH 1

DI u32 cvtpk(float lo, float hi) { f32x2 v = {lo, hi}; bf16x2 b = __builtin_convertvector(v, bf16x2); return __builtin_bit_cast(u32, b); }
DI float blo(u32 x) { return __uint_as_float(x << 16); }
DI float bhi(u32 x) { return __uint_as_float(x & 0xffff0000u); }
DI float rb(float x) { return blo(cvtpk(x, x)); }
DI float bf(u16 x) { return __uint_as_float((u32)x << 16); }
DI f32x4 mfma(u32x4 a, u32x4 b, f32x4 c) {
  return __builtin_amdgcn_mfma_f32_16x16x32_bf16(__builtin_bit_cast(bf16x8, a), __builtin_bit_cast(bf16x8, b), c, 0, 0, 0);
}
typedef LDS char* lptr;
DI u32x2 lds_r64(lptr p) { return *(LDS u32x2*)p; }
DI u32x4 lds_r128(lptr p) { return *(LDS u32x4*)p; }
DI void lds_w32(lptr p, u32 v) { *(LDS u32*)p = v; }
DI void lds_w16(lptr p, u16 v) { *(LDS u16*)p = v; }
DI void lds_w64(lptr p, u32x2 v) { *(LDS u32x2*)p = v; }
DI void lds_w128(lptr p, u32x4 v) { *(LDS u32x4*)p = v; }
DI u32x2 lds_tr(lptr p) {
  typedef short v4i16_t __attribute__((ext_vector_type(4)));
  return __builtin_bit_cast(u32x2, __builtin_amdgcn_ds_read_tr16_b64_v4i16((LDS v4i16_t*)p));
}
DI void wsync() { __builtin_amdgcn_fence(__ATOMIC_ACQ_REL, "wavefront"); __builtin_amdgcn_wave_barrier(); }
DI u32 perm(u32 hi, u32 lo, u32 sel) { return __builtin_amdgcn_perm(hi, lo, sel); }

#define STAMP(i)
struct cplx { f32x2 r, i; };
DI cplx cmul(cplx a, cplx b) { cplx c; c.r = a.r * b.r - a.i * b.i; c.i = a.r * b.i + a.i * b.r; return c; }
DI cplx csq(cplx a) { cplx c; c.r = a.r * a.r - a.i * a.i; f32x2 t = a.r * a.i; c.i = t + t; return c; }
DI cplx conj(cplx a) { cplx c; c.r = a.r; c.i = -a.i; return c; }
DI cplx cexp_p(f32x2 zr2, f32x2 zi_rev, float p) {
  cplx c;
#pragma unroll
  for (int m = 0; m < 2; m++) {
    float mag = __builtin_amdgcn_exp2f(zr2[m] * p);
    float ph = __builtin_amdgcn_fractf(zi_rev[m] * p);
    c.r[m] = mag * __builtin_amdgcn_cosf(ph);
    c.i[m] = mag * __builtin_amdgcn_sinf(ph);
  }
  return c;
}
DI void tput(lptr trow, cplx e) { u32x2 v; v[0] = cvtpk(e.r[0], e.i[0]); v[1] = cvtpk(e.r[1], e.i[1]); lds_w64(trow, v); }

typedef __amdgpu_buffer_rsrc_t rsrc_t;
DI rsrc_t mkrsrc(const void* p, u32 bytes) { return __builtin_amdgcn_make_buffer_rsrc((void*)p, 0, bytes, 0x00020000); }
DI u32 bl16(rsrc_t r, u32 off) { return __builtin_amdgcn_raw_buffer_load_b16(r, off, 0, 0); }
DI u32 bl32(rsrc_t r, u32 off) { return __builtin_amdgcn_raw_buffer_load_b32(r, off, 0, 0); }
DI u32x2 bl64(rsrc_t r, u32 off) { return __builtin_bit_cast(u32x2, __builtin_amdgcn_raw_buffer_load_b64(r, off, 0, 0)); }
DI u32x4 bl128(rsrc_t r, u32 off) { return __builtin_bit_cast(u32x4, __builtin_amdgcn_raw_buffer_load_b128(r, off, 0, 0)); }
constexpr u32 OOB = 0x80000000u;

template <int OFF> DI void rdB(u32x2& lo, u32x2& hi, u32 a) {
  asm volatile("ds_read_b64 %0, %2 offset:%3\n\tds_read_b64 %1, %2 offset:%4" : "=&v"(lo), "=&v"(hi) : "v"(a), "i"(OFF), "i"(OFF + 8) : "memory");
}
struct BF { u32x2 lo[4], hi[4]; };
template <int SG> DI void loadB(BF& b, u32 xa) {
  rdB<0 * XP_STRIDE + SG * 64>(b.lo[0], b.hi[0], xa);
  rdB<1 * XP_STRIDE + SG * 64>(b.lo[1], b.hi[1], xa);
  rdB<2 * XP_STRIDE + SG * 64>(b.lo[2], b.hi[2], xa);
  rdB<3 * XP_STRIDE + SG * 64>(b.lo[3], b.hi[3], xa);
}
DI void waitB(BF& b) {
  asm volatile("s_waitcnt lgkmcnt(0)" : "+v"(b.lo[0]), "+v"(b.hi[0]), "+v"(b.lo[1]), "+v"(b.hi[1]), "+v"(b.lo[2]), "+v"(b.hi[2]), "+v"(b.lo[3]), "+v"(b.hi[3]) :: "memory");
}
typedef f32x4 Acc[2][4][2];
template <int SG, int P0, int P1> DI void groupMM(const u32x4 (&A)[16], const BF& b, Acc& acc) {
#pragma unroll
  for (int r = 0; r < 4; r++) {
    u32x4 B = {b.lo[r][0], b.lo[r][1], b.hi[r][0], b.hi[r][1]};
#pragma unroll
    for (int sl = 0; sl < 2; sl++) {
      const int P = sl ? P1 : P0;
      if (P < 0) continue;
#pragma unroll
      for (int hh = 0; hh < 2; hh++) {
        const int c = 2 * (2 * P + hh) - (SG - 1);
        if (c >= 0) acc[sl][r][hh] = mfma(A[c], B, SG == 0 ? f32x4{0, 0, 0, 0} : acc[sl][r][hh]);
      }
    }
  }
}
template <int S, int P0, int P1, bool PRE, int NEXT> DI void block(const u32x4 (&A)[16], Acc& acc, BF& b0, u32 xa) {
  BF b1;
  if (!PRE) { loadB<4 * S>(b0, xa); waitB(b0); }
  loadB<4 * S + 1>(b1, xa);
  __builtin_amdgcn_sched_barrier(0);
  groupMM<4 * S, P0, P1>(A, b0, acc);
  __builtin_amdgcn_sched_barrier(0);
  waitB(b1);
  loadB<4 * S + 2>(b0, xa);
  __builtin_amdgcn_sched_barrier(0);
  groupMM<4 * S + 1, P0, P1>(A, b1, acc);
  __builtin_amdgcn_sched_barrier(0);
  waitB(b0);
  loadB<4 * S + 3>(b1, xa);
  __builtin_amdgcn_sched_barrier(0);
  groupMM<4 * S + 2, P0, P1>(A, b0, acc);
  __builtin_amdgcn_sched_barrier(0);
  waitB(b1);
  if (NEXT >= 0) loadB<(NEXT >= 0 ? NEXT : 0)>(b0, xa);
  __builtin_amdgcn_sched_barrier(0);
  groupMM<4 * S + 3, P0, P1>(A, b1, acc);
  __builtin_amdgcn_sched_barrier(0);
  if (NEXT >= 0) waitB(b0);
}
template <int P> DI void finish(const f32x4 (&a)[4][2], float (&carry)[3], lptr yw, int rot, bool l0) {
  float t[2][3];
#pragma unroll
  for (int hh = 0; hh < 2; hh++) {
    float c0 = a[1][hh][3] + a[2][hh][2] + a[3][hh][1], c1 = a[2][hh][3] + a[3][hh][2], c2 = a[3][hh][3];
    t[hh][0] = __int_as_float(__builtin_amdgcn_ds_bpermute(rot, __float_as_int(c0)));
    t[hh][1] = __int_as_float(__builtin_amdgcn_ds_bpermute(rot, __float_as_int(c1)));
    t[hh][2] = __int_as_float(__builtin_amdgcn_ds_bpermute(rot, __float_as_int(c2)));
  }
#pragma unroll
  for (int hh = 0; hh < 2; hh++) {
    float x[3];
#pragma unroll
    for (int k = 0; k < 3; k++) x[k] = l0 ? (hh ? t[0][k] : (P ? carry[k] : 0.f)) : t[hh][k];
    float y0 = a[0][hh][0] + x[0], y1 = a[0][hh][1] + a[1][hh][0] + x[1];
    float y2 = a[0][hh][2] + a[1][hh][1] + a[2][hh][0] + x[2], y3 = a[0][hh][3] + a[1][hh][2] + a[2][hh][1] + a[3][hh][0];
    lds_w64(yw + 512 * (2 * P + hh), u32x2{cvtpk(y0, y1), cvtpk(y2, y3)});
  }
#pragma unroll
  for (int k = 0; k < 3; k++) carry[k] = t[1][k];
}

DI void cnt_signal(lptr p, int lane) {
  asm volatile("s_waitcnt lgkmcnt(0)" ::: "memory");
  if (lane == 0) __hip_atomic_fetch_add((LDS u32*)p, 1u, __ATOMIC_RELAXED, __HIP_MEMORY_SCOPE_WORKGROUP);
  asm volatile("" ::: "memory");
}
DI void cnt_wait(lptr p, u32 target) {
  asm volatile("" ::: "memory");
#pragma unroll 1
  for (int it = 0; it < (1 << 22); it++) {
    u32 v = __builtin_amdgcn_readfirstlane(__hip_atomic_load((LDS u32*)p, __ATOMIC_RELAXED, __HIP_MEMORY_SCOPE_WORKGROUP));
    if (v >= target) break;
    __builtin_amdgcn_s_sleep(1);
  }
  asm volatile("" ::: "memory");
}

struct XIn { u32x2 k[2], v[2], h; };
DI void process_x(lptr xw, u32 sel0, u32 sel1, bool odd2, const u32x2 (&xu)[2], const XIn& in, u32x4 shw0, u32x4 shw1,
                  u32x2 (&xkv)[2]) {
  u32x2 kr[5];
#pragma unroll
  for (int d = 0; d < 2; d++) {
    u32 h1 = __builtin_amdgcn_update_dpp(0u, in.h[d], 0x101, 0xf, 0xf, true);
    u32 h2 = __builtin_amdgcn_update_dpp(0u, in.h[d], 0x102, 0xf, 0xf, true);
    kr[1][d] = __builtin_amdgcn_update_dpp(h1, in.k[0][d], 0x138, 0xf, 0xf, false);
    kr[2][d] = __builtin_amdgcn_update_dpp(h2, in.k[1][d], 0x138, 0xf, 0xf, false);
    kr[0][d] = __builtin_amdgcn_update_dpp(in.h[d], kr[2][d], 0x138, 0xf, 0xf, false);
    kr[3][d] = in.k[0][d]; kr[4][d] = in.k[1][d];
  }
#pragma unroll
  for (int cp = 0; cp < 2; cp++) {
    u32x4 sw = cp ? shw1 : shw0;
    f32x2 t0 = {blo(sw[0]), blo(sw[2])}, t1 = {bhi(sw[0]), bhi(sw[2])}, t2 = {blo(sw[1]), blo(sw[3])}, t3 = {bhi(sw[1]), bhi(sw[3])};
    f32x2 kf[5];
#pragma unroll
    for (int e = 0; e < 5; e++) kf[e] = f32x2{blo(kr[e][cp]), bhi(kr[e][cp])};
#pragma unroll
    for (int e = 0; e < 2; e++) {
      f32x2 p3 = kf[e] * t3, p2 = kf[e + 1] * t2, p1 = kf[e + 2] * t1, p0 = kf[e + 3] * t0;
      u32 u3 = cvtpk(p3[0], p3[1]), u2 = cvtpk(p2[0], p2[1]), u1 = cvtpk(p1[0], p1[1]), u0 = cvtpk(p0[0], p0[1]);
      f32x2 s = f32x2{blo(u3), bhi(u3)} + f32x2{blo(u2), bhi(u2)};
      u32 us = cvtpk(s[0], s[1]);
      s = f32x2{blo(us), bhi(us)} + f32x2{blo(u1), bhi(u1)}; us = cvtpk(s[0], s[1]);
      s = f32x2{blo(us), bhi(us)} + f32x2{blo(u0), bhi(u0)}; us = cvtpk(s[0], s[1]);
      s = f32x2{blo(us), bhi(us)} * f32x2{blo(in.v[e][cp]), bhi(in.v[e][cp])};
      xkv[e][cp] = cvtpk(s[0], s[1]);
    }
  }
#pragma unroll
  for (int arr = 0; arr < 2; arr++) {
#pragma unroll
    for (int a = 0; a < 2; a++) {
      u32x2 x = arr ? xkv[a] : xu[a];
      u32 keep = odd2 ? x[1] : x[0], send = odd2 ? x[0] : x[1];
      u32 other = __builtin_amdgcn_update_dpp(0u, send, 0x4e, 0xf, 0xf, true);
      lds_w32(xw + arr * 4 * XB_STRIDE + a * XP_STRIDE, perm(other, keep, sel0));
      lds_w32(xw + (arr * 4 + 1) * XB_STRIDE + a * XP_STRIDE, perm(other, keep, sel1));
    }
  }
}

DI void epilogue(lptr yr, u32 ooff, u32 tag, const u64* gp, u64 gr, const u32x2 (&xu)[2], const u32x2 (&xkv)[2], u32x2 dsk,
                 u32x2 dss, rsrc_t rout) {
  u32 ya[4], yb[4];
#pragma unroll
  for (int c = 0; c < 4; c++) {
    ya[c] = *(LDS u32*)(yr + c * KB_STRIDE);
    yb[c] = *(LDS u32*)(yr + (4 + c) * KB_STRIDE);
  }
  if (!__all((u32)(gr >> 32) == tag)) {
#pragma unroll 1
    for (int it = 0; it < (1 << 16); it++) {
      __builtin_amdgcn_s_sleep(2);
      gr = __hip_atomic_load(gp, __ATOMIC_RELAXED, __HIP_MEMORY_SCOPE_AGENT);
      if (__all((u32)(gr >> 32) == tag)) break;
    }
  }
#pragma unroll
  for (int e = 0; e < 2; e++) {
    float g = e ? bhi((u32)gr) : blo((u32)gr), g1 = rb(1.f - g);
    u32x2 o;
#pragma unroll
    for (int cp = 0; cp < 2; cp++) {
      float res[2];
#pragma unroll
      for (int hl = 0; hl < 2; hl++) {
        int c = cp * 2 + hl;
        float uu = hl ? bhi(xu[e][cp]) : blo(xu[e][cp]);
        float kv = hl ? bhi(xkv[e][cp]) : blo(xkv[e][cp]);
        float d1 = hl ? bhi(dsk[cp]) : blo(dsk[cp]);
        float d2 = hl ? bhi(dss[cp]) : blo(dss[cp]);
        float y1 = e ? bhi(ya[c]) : blo(ya[c]);
        float y2 = e ? bhi(yb[c]) : blo(yb[c]);
        float a = rb(y1 + rb(uu * d1));
        float bb = rb(uu * rb(y2 + rb(kv * d2)));
        res[hl] = rb(g * a) + rb(g1 * bb);
      }
      o[cp] = cvtpk(res[0], res[1]);
    }
    __builtin_amdgcn_raw_buffer_store_b64(__builtin_bit_cast(__attribute__((ext_vector_type(2))) unsigned, o), rout, ooff + e * 2048, 0, 0);
  }
}

template <class F> DI void conv(const u32x4 (&A)[16], u32 xa, lptr yw, int rot, int lane, lptr cnt, F hook) {
  Acc acc; BF b0; float carry[3];
  const bool l0 = lane == 0;
  cnt_wait(cnt + 0, 4);
  block<0, 0, 1, false, 0>(A, acc, b0, xa);
  finish<0>(acc[0], carry, yw, rot, l0);
  cnt_signal(cnt + 16, lane);
  hook(std::integral_constant<int, 0>{});
  block<0, 2, -1, true, -1>(A, acc, b0, xa);
  hook(std::integral_constant<int, 1>{});
  cnt_wait(cnt + 4, 4);
  block<1, 2, 1, false, 0>(A, acc, b0, xa);
  finish<1>(acc[1], carry, yw, rot, l0);
  cnt_signal(cnt + 20, lane);
  hook(std::integral_constant<int, 2>{});
  block<0, -1, 3, true, 4>(A, acc, b0, xa);
  hook(std::integral_constant<int, 3>{});
  block<1, -1, 3, true, -1>(A, acc, b0, xa);
  hook(std::integral_constant<int, 4>{});
  cnt_wait(cnt + 8, 4);
  block<2, 2, 3, false, -1>(A, acc, b0, xa);
  finish<2>(acc[0], carry, yw, rot, l0);
  cnt_signal(cnt + 24, lane);
  hook(std::integral_constant<int, 5>{});
  cnt_wait(cnt + 12, 4);
  block<3, -1, 3, false, -1>(A, acc, b0, xa);
  finish<3>(acc[1], carry, yw, rot, l0);
  cnt_signal(cnt + 28, lane);
}

__global__ __launch_bounds__(512, 2) void conv_kernel(
    const void* __restrict__ gu, const void* __restrict__ logAr, const void* __restrict__ Aim, const void* __restrict__ Cre,
    const void* __restrict__ Cim, const void* __restrict__ logdt, const void* __restrict__ Dskip, const void* __restrict__ gk,
    const void* __restrict__ gv, const void* __restrict__ shk, const void* __restrict__ ssmk, const void* __restrict__ ssmD,
    const void* __restrict__ gatew, void* ws, void* __restrict__ out) {
  extern __shared__ __attribute__((aligned(16))) char smem_[];
  lptr smem = (lptr)smem_;
  const int b = blockIdx.x, cg = (b & 7) * 32 + (b >> 3);
  const int tid = threadIdx.x, lane = tid & 63, w = __builtin_amdgcn_readfirstlane(tid >> 6);
  const int ch = cg * 4 + (w & 3);
  const int j16 = lane & 15, q = lane >> 4;
  const bool isA = w < 4;
  lptr kb = smem + OFF_K + w * KB_STRIDE;
  u64* gq = (u64*)ws;
  u32* gepoch = (u32*)((char*)ws + 65536);
  const rsrc_t ru = mkrsrc(gu, L * D * 2), rk = mkrsrc(gk, L * D * 2), rv = mkrsrc(gv, L * D * 2), rout = mkrsrc(out, L * D * 2);
  STAMP(0);
  lptr cnt = smem + OFF_MISC;
  u64 pc;
  asm volatile("s_getpc_b64 %0" : "=s"(pc));
  if (tid < 16) lds_w32(cnt + tid * 4, 0);
  asm volatile("s_waitcnt lgkmcnt(0)\n\ts_barrier" ::: "memory");
  auto loadA = [&](u32x4 (&A)[16]) {
    const int m = lane & 3, ep = (lane >> 2) & 3;
    lptr ka = kb + 32 + (-12 + 4 * m + 4 * (8 * q + 7 - ep)) * 2;
#pragma unroll
    for (int c = 0; c < 16; c++) {
      u32x2 lo = lds_tr(ka + c * 256), hi = lds_tr(ka + c * 256 - 32);
      A[c] = u32x4{lo[0], lo[1], hi[0], hi[1]};
    }
  };
  const u32 xa = (u32)(size_t)(smem + OFF_X + w * XB_STRIDE + (64 - 32 + 4 * j16 - 8 * q - 4) * 2);
  const int rot = (lane == 0 ? 63 : q == 0 ? lane + 47 : lane - 16) * 4;
  lptr yw = kb + 32 + (16 * j16 + 4 * q) * 2;
  u32x4 A[16];
  const int tb = tid & 255, wb = w & 3;
  auto load_par = [&]() { return bl32(mkrsrc(shk, D * 8), lane < 8 ? cg * 32 + lane * 4 : OOB) | bl32(mkrsrc(Dskip, D * 2), (lane >> 1) == 4 ? cg * 8 + (lane & 1) * 4 : OOB) |
                  bl32(mkrsrc(ssmD, D * 2), (lane >> 1) == 5 ? cg * 8 + (lane & 1) * 4 : OOB); };
  const u32 xoff = (2 * tb * 256 + cg) * 8;
  u32x2 xu[4][2], xkv[4][2];
  XIn in[4];
  auto load_x = [&](int s) {
    const u32 o = xoff + s * 512 * 2048;
    const int hrow = 512 * s + 128 * wb - 3 + lane;
    in[s].k[0] = bl64(rk, o); in[s].k[1] = bl64(rk, o + 2048);
    in[s].h = bl64(rk, (lane < 3 && hrow >= 0) ? (u32)(hrow * 256 + cg) * 8 : OOB);
    in[s].v[0] = bl64(rv, o); in[s].v[1] = bl64(rv, o + 2048);
    xu[s][0] = bl64(ru, o); xu[s][1] = bl64(ru, o + 2048);
    __builtin_amdgcn_sched_barrier(0);
  };
  auto landed = [&](int s) { asm volatile("" : "+v"(xu[s][1])); __builtin_amdgcn_sched_barrier(0); };
  const bool odd2 = lane & 2;
  const u32 sel0 = odd2 ? 0x01000504u : 0x05040100u, sel1 = odd2 ? 0x03020706u : 0x07060302u;
  lptr xw = smem + OFF_X + (odd2 ? 2 * XB_STRIDE : 0) + (lane & 1) * 2 * XP_STRIDE + 128 + (tb >> 2) * 4;
  u32x4 shw0, shw1; u32x2 dsk, dss;
  auto unpack_par = [&](u32 par) {
#pragma unroll
    for (int e = 0; e < 4; e++) { shw0[e] = __builtin_amdgcn_readlane(par, e); shw1[e] = __builtin_amdgcn_readlane(par, 4 + e); }
#pragma unroll
    for (int e = 0; e < 2; e++) { dsk[e] = __builtin_amdgcn_readlane(par, 8 + e); dss[e] = __builtin_amdgcn_readlane(par, 10 + e); }
  };
  auto proc = [&](int s) {
    process_x(xw + s * 256, sel0, sel1, odd2, xu[s], in[s], shw0, shw1, xkv[s]);
    cnt_signal(cnt + 4 * s, lane);
  };
  lptr yr = smem + OFF_K + 32 + tb * 4;
  const u64* gp = gq + tb;
  u64 gr[4];
  auto ldg = [&](int s) { gr[s] = __hip_atomic_load(gp + s * 256, __ATOMIC_RELAXED, __HIP_MEMORY_SCOPE_AGENT); };
  auto epi = [&](int s, u32 tag) {
    cnt_wait(cnt + 16 + 4 * s, 8);
    epilogue(yr + s * 1024, xoff + s * 512 * 2048, tag, gp + s * 256, gr[s], xu[s], xkv[s], dsk, dss, rout);
  };
  auto retire = [&](u32 tag) {
    if (b == 0 && lane == 0) {
      u32 n = __hip_atomic_fetch_add((LDS u32*)(cnt + 32), 1u, __ATOMIC_RELAXED, __HIP_MEMORY_SCOPE_WORKGROUP);
      if (n == 7) __hip_atomic_store(gepoch, tag, __ATOMIC_RELAXED, __HIP_MEMORY_SCOPE_AGENT);
    }
  };

  if (isA) {
    const rsrc_t r1 = mkrsrc(logAr, D * NM * 2), r2 = mkrsrc(Aim, D * NM * 2), r3 = mkrsrc(Cre, D * NM * 2), r4 = mkrsrc(Cim, D * NM * 2);
    const u32 po = (ch * 64 + lane) * 4;
    u32 pAr = bl32(r1, po), pAi = bl32(r2, po), pCr = bl32(r3, po), pCi = bl32(r4, po);
    u32 pdt = bl16(mkrsrc(logdt, D * 2), ch * 2);
    const u32 par = load_par();
    if (w == 0) {
      __builtin_amdgcn_raw_ptr_buffer_load_lds(mkrsrc((const void*)(pc & ~127ull), CODE_BYTES), (LDS void*)(smem + OFF_PF + 4096), 4,
                                               lane < 8 ? ((b >> 3) * 8 + lane) * 128 : OOB, 0, 0, 0);
    }
    {
      const u32 pf = ((b >> 3) * 64 + (tid >> 4)) * 2048 + (b & 7) * 256 + (tid & 15) * 16;
      LDS void* sink = (LDS void*)(smem + OFF_PF + w * 1024);
#pragma unroll
      for (int i = 0; i < 4; i++) {
        __builtin_amdgcn_raw_ptr_buffer_load_lds(rk, sink, 16, pf + i * 16 * 2048, 0, 0, 0);
        __builtin_amdgcn_raw_ptr_buffer_load_lds(rv, sink, 16, pf + i * 16 * 2048, 0, 0, 0);
        __builtin_amdgcn_raw_ptr_buffer_load_lds(ru, sink, 16, pf + i * 16 * 2048, 0, 0, 0);
      }
    }
    STAMP(10);
    float dt = rb(expf(bf(pdt)));
    f32x2 zr, zi; cplx C;
    zr[0] = -rb(expf(blo(pAr))) * dt; zr[1] = -rb(expf(bhi(pAr))) * dt;
    zi[0] = blo(pAi) * dt; zi[1] = bhi(pAi) * dt;
    C.r[0] = 2.f * blo(pCr) * dt; C.r[1] = 2.f * bhi(pCr) * dt;
    C.i[0] = -2.f * blo(pCi) * dt; C.i[1] = -2.f * bhi(pCi) * dt;
    zr *= 1.44269504088896341f; zi *= 0.15915494309189535f;
    STAMP(11);
    lptr tb = smem + OFF_T + w * TB_STRIDE;
    lptr tw = tb + lane * 8, tr = tb + j16 * TB_ROW + q * 16;
    cplx w1 = cexp_p(zr, zi, 1.f), w16 = cexp_p(zr, zi, 16.f), w256 = cexp_p(zr, zi, 256.f);
    u32x4 G[8];
    {
      cplx g; g.r = f32x2{1.f, 1.f}; g.i = f32x2{0.f, 0.f};
      tput(tw, g); g = w16; tput(tw + TB_ROW, g);
#pragma unroll
      for (int i = 2; i < 16; i++) { g = cmul(g, w16); tput(tw + i * TB_ROW, g); }
      wsync();
#pragma unroll
      for (int s = 0; s < 8; s++) G[s] = lds_r128(tr + s * 64);
      wsync();
    }
    STAMP(12);
    w1 = conj(w1); w256 = conj(w256);
    cplx w2 = csq(w1), w4 = csq(w2), w8 = csq(w4), w512 = csq(w256), w1024 = csq(w512);
    lptr kd = kb + 32;
    if (lane < 8) lds_w32(kb + lane * 4, 0);
    {
      cplx e = C; tput(tw, e);
#pragma unroll
      for (int i = 1; i < 16; i++) { e = cmul(e, w1); tput(tw + i * TB_ROW, e); }
      f32x4 a = {0, 0, 0, 0};
      wsync();
#pragma unroll
      for (int s = 0; s < 8; s++) a = mfma(lds_r128(tr + s * 64), G[s], a);
      wsync();
      lds_w64(kd + (16 * j16 + 4 * q) * 2, u32x2{cvtpk(a[0], a[1]), cvtpk(a[2], a[3])});
    }
    STAMP(13);
    {
      cplx e = cmul(C, w256); tput(tw, e);
#pragma unroll
      for (int i = 1; i < 8; i++) { e = cmul(e, w2); tput(tw + i * TB_ROW, e); }
      e = cmul(C, w512);
#pragma unroll
      for (int i = 0; i < 4; i++) { tput(tw + (8 + i) * TB_ROW, e); tput(tw + (12 + i) * TB_ROW, cmul(e, w256)); if (i < 3) e = cmul(e, w4); }
      f32x4 a = {0, 0, 0, 0};
      wsync();
#pragma unroll
      for (int s = 0; s < 8; s++) a = mfma(lds_r128(tr + s * 64), G[s], a);
      wsync();
      u32 p01 = cvtpk(a[0], a[1]), p23 = cvtpk(a[2], a[3]);
      u32 v00 = perm(p01, p01, 0x01000100), v11 = perm(p01, p01, 0x03020302), v22 = perm(p23, p23, 0x01000100), v33 = perm(p23, p23, 0x03020302);
      const int base = q < 2 ? 256 + 16 * j16 + 8 * q : 512 + 256 * (q - 2) + 16 * j16;
      lds_w128(kd + base * 2, q < 2 ? u32x4{v00, perm(p23, p01, 0x05040302), v22, v33} : u32x4{v00, p01, v11, v22});
      if (q >= 2) lds_w128(kd + base * 2 + 16, u32x4{v22, p23, v33, v33});
      wsync();
      if (q >= 2 && base > 512) lds_w32(kd + (base - 2) * 2, v00); else if (base > 256) lds_w16(kd + (base - 1) * 2, (u16)p01);
      wsync();
    }
    STAMP(14);
    load_x(3);
    {
      cplx e0 = cmul(C, w1024), e1 = cmul(e0, w8);
#pragma unroll
      for (int i = 0; i < 4; i++) { tput(tw + (2 * i) * TB_ROW, e0); tput(tw + (2 * i + 1) * TB_ROW, e1); e0 = cmul(e0, w256); if (i < 3) e1 = cmul(e1, w256); }
      tput(tw + 8 * TB_ROW, e0);
      f32x4 a = {0, 0, 0, 0};
      wsync();
#pragma unroll
      for (int s = 0; s < 8; s++) a = mfma(lds_r128(tr + s * 64), G[s], a);
      wsync();
      u32 p01 = cvtpk(a[0], a[1]), p23 = cvtpk(a[2], a[3]);
      if (q < 2) {
#pragma unroll
        for (int hh = 0; hh < 2; hh++) {
          u32 p = hh ? p23 : p01;
          u32 v00 = perm(p, p, 0x01000100), v11 = perm(p, p, 0x03020302);
          int base = 1024 + 512 * q + 256 * hh + 16 * j16;
          lds_w128(kd + base * 2, u32x4{v00, v00, p, v11});
          lds_w128(kd + base * 2 + 16, u32x4{v11, v11, v11, v11});
        }
      }
      wsync();
      if (q < 2) {
#pragma unroll
        for (int hh = 0; hh < 2; hh++) {
          u32 p = hh ? p23 : p01;
          u32 v00 = perm(p, p, 0x01000100);
          int base = 1024 + 512 * q + 256 * hh + 16 * j16;
          if (base > 1024) lds_w64(kd + (base - 4) * 2, u32x2{v00, v00}); else lds_w32(kd + (base - 2) * 2, v00);
        }
      } else if (lane == 32) {
        u32 v00 = perm(p01, p01, 0x01000100);
        lds_w64(kd + 2044 * 2, u32x2{v00, v00});
      }
      wsync();
    }
    STAMP(15);
    loadA(A);
    STAMP(1);
    u32 tag = 0;
    conv(A, xa, yw, rot, lane, cnt, [&](auto k) {
      constexpr int K = k.value;
      STAMP(2 + K);
      if (K == 1) { unpack_par(par); proc(3); }
      if (K == 4) { tag = __builtin_amdgcn_readfirstlane(*(LDS u32*)(cnt + 36)); ldg(3); }
    });
    STAMP(8);
    epi(3, tag);
    retire(tag);
    STAMP(9);
  } else {
    const rsrc_t rs = mkrsrc(ssmk, L * D * 2), rw = mkrsrc(gatew, D * 2);
    const u32 par = load_par();
    load_x(0);
    const u32 epoch = __hip_atomic_load(gepoch, __ATOMIC_RELAXED, __HIP_MEMORY_SCOPE_AGENT);
    const int grow = b * 8 + wb * 2;
    u32x4 gx[4], gw[2], kk[4];
#pragma unroll
    for (int e = 0; e < 4; e++) kk[e] = bl128(rs, ch * 4096 + (e * 64 + lane) * 16);
#pragma unroll
    for (int e = 0; e < 4; e++) gx[e] = bl128(ru, grow * 2048 + (e * 64 + lane) * 16);
#pragma unroll
    for (int e = 0; e < 2; e++) gw[e] = bl128(rw, (e * 64 + lane) * 16);
    STAMP(10);
    for (int i = tb; i < 32 * 32; i += 256) lds_w32(smem + OFF_X + (i >> 7) * XB_STRIDE + ((i >> 5) & 3) * XP_STRIDE + (i & 31) * 4, 0);
    if (lane < 8) lds_w32(kb + lane * 4, 0);
    const u32 tag = epoch + 1;
    auto gate = [&]() {
      u32 gb[2];
#pragma unroll
      for (int rr = 0; rr < 2; rr++) {
        float s = 0.f;
#pragma unroll
        for (int h = 0; h < 2; h++) {
          u32x4 x = gx[rr * 2 + h], ww = gw[h];
#pragma unroll
          for (int e = 0; e < 4; e++) { u32 p = cvtpk(blo(x[e]) * blo(ww[e]), bhi(x[e]) * bhi(ww[e])); s += blo(p) + bhi(p); }
        }
#pragma unroll
        for (int o = 32; o >= 1; o >>= 1) s += __shfl_xor(s, o);
        float z = rb(s), g = 1.f / (1.f + expf(-z));
        gb[rr] = cvtpk(g, g) & 0xffffu;
      }
      if (lane == 0) __hip_atomic_store(gq + (grow >> 1), ((u64)tag << 32) | (gb[0] | gb[1] << 16), __ATOMIC_RELAXED, __HIP_MEMORY_SCOPE_AGENT);
    };
    STAMP(11);
    landed(0);
    unpack_par(par);
    if (lane == 0) lds_w32(cnt + 36, tag);
    STAMP(12);
    load_x(1);
    STAMP(13);
    proc(0);
    STAMP(14);
#pragma unroll
    for (int e = 0; e < 4; e++) lds_w128(kb + 32 + (e * 64 + lane) * 16, kk[e]);
    STAMP(15);
    wsync();
    loadA(A);
    STAMP(1);
    conv(A, xa, yw, rot, lane, cnt, [&](auto k) {
      constexpr int K = k.value;
      STAMP(2 + K);
      if (K == 0) { landed(1); load_x(2); proc(1); }
      if (K == 1) gate();
      if (K == 2) proc(2);
      if (K == 3) { ldg(0); ldg(1); }
      if (K == 4) { epi(0, tag); ldg(2); }
      if (K == 5) epi(1, tag);
    });
    STAMP(8);
    epi(2, tag);
    retire(tag);
    STAMP(9);
  }
}

extern "C" void kernel_launch(void* const* d_in, const int* in_sizes, int n_in, void* d_out, int out_size, void* d_ws,
                              size_t ws_size, hipStream_t stream) {
  (void)hipFuncSetAttribute((const void*)conv_kernel, hipFuncAttributeMaxDynamicSharedMemorySize, LDS_BYTES);
  conv_kernel<<<256, 512, LDS_BYTES, stream>>>(d_in[0], d_in[1], d_in[2], d_in[3], d_in[4], d_in[5], d_in[6], d_in[8], d_in[9],
                                               d_in[10], d_in[11], d_in[12], d_in[13], d_ws, d_out);
}
```

```cpp
#include <hip/hip_runtime.h>
#include <hip/hip_bf16.h>
#include <type_traits>

typedef unsigned int u32;
typedef unsigned short u16;
typedef unsigned long long u64;
typedef u32 u32x2 __attribute__((ext_vector_type(2)));
typedef u32 u32x4 __attribute__((ext_vector_type(4)));
typedef float f32x2 __attribute__((ext_vector_type(2)));
typedef float f32x4 __attribute__((ext_vector_type(4)));
typedef __bf16 bf16x2 __attribute__((ext_vector_type(2)));
typedef __bf16 bf16x8 __attribute__((ext_vector_type(8)));
typedef short s16x4 __attribute__((ext_vector_type(4)));
#define DI __device__ __forceinline__
#define LDS __attribute__((address_space(3)))

constexpr int L = 2048, D = 1024, NM = 128;
constexpr int KB_STRIDE = 4128;
constexpr int XP_STRIDE = 1184;
constexpr int XB_STRIDE = 4 * XP_STRIDE + 64;
constexpr int TB_ROW = 528;
constexpr int TB_STRIDE = 16 * TB_ROW;
constexpr int OFF_K = 0;
constexpr int OFF_X = OFF_K + 8 * KB_STRIDE;
constexpr int OFF_T = OFF_X + 8 * XB_STRIDE;
constexpr int OFF_MISC = OFF_T + 4 * TB_STRIDE;
constexpr int OFF_PF = OFF_MISC + 64;
constexpr int LDS_BYTES = OFF_PF + 4096 + 256;
constexpr int CODE_BYTES = 32768;
#define GATE_EARLY 0
#define CODEPF 1
#define PREFETCH 0

DI u32 cvtpk(float lo, float hi) { f32x2 v = {lo, hi}; bf16x2 b = __builtin_convertvector(v, bf16x2); return __builtin_bit_cast(u32, b); }
DI float blo(u32 x) { return __uint_as_float(x << 16); }
DI float bhi(u32 x) { return __uint_as_float(x & 0xffff0000u); }
DI float rb(float x) { return blo(cvtpk(x, x)); }
DI float bf(u16 x) { return __uint_as_float((u32)x << 16); }
DI f32x4 mfma(u32x4 a, u32x4 b, f32x4 c) {
  return __builtin_amdgcn_mfma_f32_16x16x32_bf16(__builtin_bit_cast(bf16x8, a), __builtin_bit_cast(bf16x8, b), c, 0, 0, 0);
}
typedef LDS char* lptr;
DI u32x2 lds_r64(lptr p) { return *(LDS u32x2*)p; }
DI u32x4 lds_r128(lptr p) { return *(LDS u32x4*)p; }
DI void lds_w32(lptr p, u32 v) { *(LDS u32*)p = v; }
DI void lds_w16(lptr p, u16 v) { *(LDS u16*)p = v; }
DI void lds_w64(lptr p, u32x2 v) { *(LDS u32x2*)p = v; }
DI void lds_w128(lptr p, u32x4 v) { *(LDS u32x4*)p = v; }
DI u32x2 lds_tr(lptr p) {
  typedef short v4i16_t __attribute__((ext_vector_type(4)));
  return __builtin_bit_cast(u32x2, __builtin_amdgcn_ds_read_tr16_b64_v4i16((LDS v4i16_t*)p));
}
DI void wsync() { __builtin_amdgcn_fence(__ATOMIC_ACQ_REL, "wavefront"); __builtin_amdgcn_wave_barrier(); }
DI u32 perm(u32 hi, u32 lo, u32 sel) { return __builtin_amdgcn_perm(hi, lo, sel); }

#define STAMP(i)
struct cplx { f32x2 r, i; };
DI cplx cmul(cplx a, cplx b) { cplx c; c.r = a.r * b.r - a.i * b.i; c.i = a.r * b.i + a.i * b.r; return c; }
DI cplx csq(cplx a) { cplx c; c.r = a.r * a.r - a.i * a.i; f32x2 t = a.r * a.i; c.i = t + t; return c; }
DI cplx conj(cplx a) { cplx c; c.r = a.r; c.i = -a.i; return c; }
DI cplx cexp_p(f32x2 zr2, f32x2 zi_rev, float p) {
  cplx c;
#pragma unroll
  for (int m = 0; m < 2; m++) {
    float mag = __builtin_amdgcn_exp2f(zr2[m] * p);
    float ph = __builtin_amdgcn_fractf(zi_rev[m] * p);
    c.r[m] = mag * __builtin_amdgcn_cosf(ph);
    c.i[m] = mag * __builtin_amdgcn_sinf(ph);
  }
  return c;
}
DI void tput(lptr trow, cplx e) { u32x2 v; v[0] = cvtpk(e.r[0], e.i[0]); v[1] = cvtpk(e.r[1], e.i[1]); lds_w64(trow, v); }

typedef __amdgpu_buffer_rsrc_t rsrc_t;
DI rsrc_t mkrsrc(const void* p, u32 bytes) { return __builtin_amdgcn_make_buffer_rsrc((void*)p, 0, bytes, 0x00020000); }
DI u32 bl16(rsrc_t r, u32 off) { return __builtin_amdgcn_raw_buffer_load_b16(r, off, 0, 0); }
DI u32 bl32(rsrc_t r, u32 off) { return __builtin_amdgcn_raw_buffer_load_b32(r, off, 0, 0); }
DI u32x2 bl64(rsrc_t r, u32 off) { return __builtin_bit_cast(u32x2, __builtin_amdgcn_raw_buffer_load_b64(r, off, 0, 0)); }
DI u32x4 bl128(rsrc_t r, u32 off) { return __builtin_bit_cast(u32x4, __builtin_amdgcn_raw_buffer_load_b128(r, off, 0, 0)); }
constexpr u32 OOB = 0x80000000u;

template <int OFF> DI void rdB(u32x2& lo, u32x2& hi, u32 a) {
  asm volatile("ds_read_b64 %0, %2 offset:%3\n\tds_read_b64 %1, %2 offset:%4" : "=&v"(lo), "=&v"(hi) : "v"(a), "i"(OFF), "i"(OFF + 8) : "memory");
}
struct BF { u32x2 lo[4], hi[4]; };
template <int SG> DI void loadB(BF& b, u32 xa) {
  rdB<0 * XP_STRIDE + SG * 64>(b.lo[0], b.hi[0], xa);
  rdB<1 * XP_STRIDE + SG * 64>(b.lo[1], b.hi[1], xa);
  rdB<2 * XP_STRIDE + SG * 64>(b.lo[2], b.hi[2], xa);
  rdB<3 * XP_STRIDE + SG * 64>(b.lo[3], b.hi[3], xa);
}
DI void waitB(BF& b) {
  asm volatile("s_waitcnt lgkmcnt(0)" : "+v"(b.lo[0]), "+v"(b.hi[0]), "+v"(b.lo[1]), "+v"(b.hi[1]), "+v"(b.lo[2]), "+v"(b.hi[2]), "+v"(b.lo[3]), "+v"(b.hi[3]) :: "memory");
}
typedef f32x4 Acc[2][4][2];
template <int SG, int P0, int P1> DI void groupMM(const u32x4 (&A)[16], const BF& b, Acc& acc) {
#pragma unroll
  for (int r = 0; r < 4; r++) {
    u32x4 B = {b.lo[r][0], b.lo[r][1], b.hi[r][0], b.hi[r][1]};
#pragma unroll
    for (int sl = 0; sl < 2; sl++) {
      const int P = sl ? P1 : P0;
      if (P < 0) continue;
#pragma unroll
      for (int hh = 0; hh < 2; hh++) {
        const int c = 2 * (2 * P + hh) - (SG - 1);
        if (c >= 0) acc[sl][r][hh] = mfma(A[c], B, SG == 0 ? f32x4{0, 0, 0, 0} : acc[sl][r][hh]);
      }
    }
  }
}
template <int S, int P0, int P1, bool PRE, int NEXT> DI void block(const u32x4 (&A)[16], Acc& acc, BF& b0, u32 xa) {
  BF b1;
  if (!PRE) { loadB<4 * S>(b0, xa); waitB(b0); }
  loadB<4 * S + 1>(b1, xa);
  __builtin_amdgcn_sched_barrier(0);
  groupMM<4 * S, P0, P1>(A, b0, acc);
  __builtin_amdgcn_sched_barrier(0);
  waitB(b1);
  loadB<4 * S + 2>(b0, xa);
  __builtin_amdgcn_sched_barrier(0);
  groupMM<4 * S + 1, P0, P1>(A, b1, acc);
  __builtin_amdgcn_sched_barrier(0);
  waitB(b0);
  loadB<4 * S + 3>(b1, xa);
  __builtin_amdgcn_sched_barrier(0);
  groupMM<4 * S + 2, P0, P1>(A, b0, acc);
  __builtin_amdgcn_sched_barrier(0);
  waitB(b1);
  if (NEXT >= 0) loadB<(NEXT >= 0 ? NEXT : 0)>(b0, xa);
  __builtin_amdgcn_sched_barrier(0);
  groupMM<4 * S + 3, P0, P1>(A, b1, acc);
  __builtin_amdgcn_sched_barrier(0);
  if (NEXT >= 0) waitB(b0);
}
template <int P> DI void finish(const f32x4 (&a)[4][2], float (&carry)[3], lptr yw, int rot, bool l0) {
  float t[2][3];
#pragma unroll
  for (int hh = 0; hh < 2; hh++) {
    float c0 = a[1][hh][3] + a[2][hh][2] + a[3][hh][1], c1 = a[2][hh][3] + a[3][hh][2], c2 = a[3][hh][3];
    t[hh][0] = __int_as_float(__builtin_amdgcn_ds_bpermute(rot, __float_as_int(c0)));
    t[hh][1] = __int_as_float(__builtin_amdgcn_ds_bpermute(rot, __float_as_int(c1)));
    t[hh][2] = __int_as_float(__builtin_amdgcn_ds_bpermute(rot, __float_as_int(c2)));
  }
#pragma unroll
  for (int hh = 0; hh < 2; hh++) {
    float x[3];
#pragma unroll
    for (int k = 0; k < 3; k++) x[k] = l0 ? (hh ? t[0][k] : (P ? carry[k] : 0.f)) : t[hh][k];
    float y0 = a[0][hh][0] + x[0], y1 = a[0][hh][1] + a[1][hh][0] + x[1];
    float y2 = a[0][hh][2] + a[1][hh][1] + a[2][hh][0] + x[2], y3 = a[0][hh][3] + a[1][hh][2] + a[2][hh][1] + a[3][hh][0];
    lds_w64(yw + 512 * (2 * P + hh), u32x2{cvtpk(y0, y1), cvtpk(y2, y3)});
  }
#pragma unroll
  for (int k = 0; k < 3; k++) carry[k] = t[1][k];
}

DI void cnt_signal(lptr p, int lane) {
  asm volatile("s_waitcnt lgkmcnt(0)" ::: "memory");
  if (lane == 0) __hip_atomic_fetch_add((LDS u32*)p, 1u, __ATOMIC_RELAXED, __HIP_MEMORY_SCOPE_WORKGROUP);
  asm volatile("" ::: "memory");
}
DI void cnt_wait(lptr p, u32 target) {
  asm volatile("" ::: "memory");
#pragma unroll 1
  for (int it = 0; it < (1 << 22); it++) {
    u32 v = __builtin_amdgcn_readfirstlane(__hip_atomic_load((LDS u32*)p, __ATOMIC_RELAXED, __HIP_MEMORY_SCOPE_WORKGROUP));
    if (v >= target) break;
    __builtin_amdgcn_s_sleep(1);
  }
  asm volatile("" ::: "memory");
}

struct XIn { u32x2 k[2], v[2], h; };
DI void process_x(lptr xw, u32 sel0, u32 sel1, bool odd2, const u32x2 (&xu)[2], const XIn& in, u32x4 shw0, u32x4 shw1,
                  u32x2 (&xkv)[2]) {
  u32x2 kr[5];
#pragma unroll
  for (int d = 0; d < 2; d++) {
    u32 h1 = __builtin_amdgcn_update_dpp(0u, in.h[d], 0x101, 0xf, 0xf, true);
    u32 h2 = __builtin_amdgcn_update_dpp(0u, in.h[d], 0x102, 0xf, 0xf, true);
    kr[1][d] = __builtin_amdgcn_update_dpp(h1, in.k[0][d], 0x138, 0xf, 0xf, false);
    kr[2][d] = __builtin_amdgcn_update_dpp(h2, in.k[1][d], 0x138, 0xf, 0xf, false);
    kr[0][d] = __builtin_amdgcn_update_dpp(in.h[d], kr[2][d], 0x138, 0xf, 0xf, false);
    kr[3][d] = in.k[0][d]; kr[4][d] = in.k[1][d];
  }
#pragma unroll
  for (int cp = 0; cp < 2; cp++) {
    u32x4 sw = cp ? shw1 : shw0;
    f32x2 t0 = {blo(sw[0]), blo(sw[2])}, t1 = {bhi(sw[0]), bhi(sw[2])}, t2 = {blo(sw[1]), blo(sw[3])}, t3 = {bhi(sw[1]), bhi(sw[3])};
    f32x2 kf[5];
#pragma unroll
    for (int e = 0; e < 5; e++) kf[e] = f32x2{blo(kr[e][cp]), bhi(kr[e][cp])};
#pragma unroll
    for (int e = 0; e < 2; e++) {
      f32x2 p3 = kf[e] * t3, p2 = kf[e + 1] * t2, p1 = kf[e + 2] * t1, p0 = kf[e + 3] * t0;
      u32 u3 = cvtpk(p3[0], p3[1]), u2 = cvtpk(p2[0], p2[1]), u1 = cvtpk(p1[0], p1[1]), u0 = cvtpk(p0[0], p0[1]);
      f32x2 s = f32x2{blo(u3), bhi(u3)} + f32x2{blo(u2), bhi(u2)};
      u32 us = cvtpk(s[0], s[1]);
      s = f32x2{blo(us), bhi(us)} + f32x2{blo(u1), bhi(u1)}; us = cvtpk(s[0], s[1]);
      s = f32x2{blo(us), bhi(us)} + f32x2{blo(u0), bhi(u0)}; us = cvtpk(s[0], s[1]);
      s = f32x2{blo(us), bhi(us)} * f32x2{blo(in.v[e][cp]), bhi(in.v[e][cp])};
      xkv[e][cp] = cvtpk(s[0], s[1]);
    }
  }
#pragma unroll
  for (int arr = 0; arr < 2; arr++) {
#pragma unroll
    for (int a = 0; a < 2; a++) {
      u32x2 x = arr ? xkv[a] : xu[a];
      u32 keep = odd2 ? x[1] : x[0], send = odd2 ? x[0] : x[1];
      u32 other = __builtin_amdgcn_update_dpp(0u, send, 0x4e, 0xf, 0xf, true);
      lds_w32(xw + arr * 4 * XB_STRIDE + a * XP_STRIDE, perm(other, keep, sel0));
      lds_w32(xw + (arr * 4 + 1) * XB_STRIDE + a * XP_STRIDE, perm(other, keep, sel1));
    }
  }
}

DI void epilogue(lptr yr, u32 ooff, u32 tag, const u64* gp, u64 gr, const u32x2 (&xu)[2], const u32x2 (&xkv)[2], u32x2 dsk,
                 u32x2 dss, rsrc_t rout) {
  u32 ya[4], yb[4];
#pragma unroll
  for (int c = 0; c < 4; c++) {
    ya[c] = *(LDS u32*)(yr + c * KB_STRIDE);
    yb[c] = *(LDS u32*)(yr + (4 + c) * KB_STRIDE);
  }
  if (!__all((u32)(gr >> 32) == tag)) {
#pragma unroll 1
    for (int it = 0; it < (1 << 16); it++) {
      __builtin_amdgcn_s_sleep(2);
      gr = __hip_atomic_load(gp, __ATOMIC_RELAXED, __HIP_MEMORY_SCOPE_AGENT);
      if (__all((u32)(gr >> 32) == tag)) break;
    }
  }
#pragma unroll
  for (int e = 0; e < 2; e++) {
    float g = e ? bhi((u32)gr) : blo((u32)gr), g1 = rb(1.f - g);
    u32x2 o;
#pragma unroll
    for (int cp = 0; cp < 2; cp++) {
      float res[2];
#pragma unroll
      for (int hl = 0; hl < 2; hl++) {
        int c = cp * 2 + hl;
        float uu = hl ? bhi(xu[e][cp]) : blo(xu[e][cp]);
        float kv = hl ? bhi(xkv[e][cp]) : blo(xkv[e][cp]);
        float d1 = hl ? bhi(dsk[cp]) : blo(dsk[cp]);
        float d2 = hl ? bhi(dss[cp]) : blo(dss[cp]);
        float y1 = e ? bhi(ya[c]) : blo(ya[c]);
        float y2 = e ? bhi(yb[c]) : blo(yb[c]);
        float a = rb(y1 + rb(uu * d1));
        float bb = rb(uu * rb(y2 + rb(kv * d2)));
        res[hl] = rb(g * a) + rb(g1 * bb);
      }
      o[cp] = cvtpk(res[0], res[1]);
    }
    __builtin_amdgcn_raw_buffer_store_b64(__builtin_bit_cast(__attribute__((ext_vector_type(2))) unsigned, o), rout, ooff + e * 2048, 0, 0);
  }
}

template <class F> DI void conv(const u32x4 (&A)[16], u32 xa, lptr yw, int rot, int lane, lptr cnt, F hook) {
  Acc acc; BF b0; float carry[3];
  const bool l0 = lane == 0;
  cnt_wait(cnt + 0, 4);
  block<0, 0, 1, false, 0>(A, acc, b0, xa);
  finish<0>(acc[0], carry, yw, rot, l0);
  cnt_signal(cnt + 16, lane);
  hook(std::integral_constant<int, 0>{});
  block<0, 2, -1, true, -1>(A, acc, b0, xa);
  hook(std::integral_constant<int, 1>{});
  cnt_wait(cnt + 4, 4);
  block<1, 2, 1, false, 0>(A, acc, b0, xa);
  finish<1>(acc[1], carry, yw, rot, l0);
  cnt_signal(cnt + 20, lane);
  hook(std::integral_constant<int, 2>{});
  block<0, -1, 3, true, 4>(A, acc, b0, xa);
  hook(std::integral_constant<int, 3>{});
  block<1, -1, 3, true, -1>(A, acc, b0, xa);
  hook(std::integral_constant<int, 4>{});
  cnt_wait(cnt + 8, 4);
  block<2, 2, 3, false, -1>(A, acc, b0, xa);
  finish<2>(acc[0], carry, yw, rot, l0);
  cnt_signal(cnt + 24, lane);
  hook(std::integral_constant<int, 5>{});
  cnt_wait(cnt + 12, 4);
  block<3, -1, 3, false, -1>(A, acc, b0, xa);
  finish<3>(acc[1], carry, yw, rot, l0);
  cnt_signal(cnt + 28, lane);
}

__global__ __launch_bounds__(512, 2) void conv_kernel(
    const void* __restrict__ gu, const void* __restrict__ logAr, const void* __restrict__ Aim, const void* __restrict__ Cre,
    const void* __restrict__ Cim, const void* __restrict__ logdt, const void* __restrict__ Dskip, const void* __restrict__ gk,
    const void* __restrict__ gv, const void* __restrict__ shk, const void* __restrict__ ssmk, const void* __restrict__ ssmD,
    const void* __restrict__ gatew, void* ws, void* __restrict__ out) {
  extern __shared__ __attribute__((aligned(16))) char smem_[];
  lptr smem = (lptr)smem_;
  const int b = blockIdx.x, cg = (b & 7) * 32 + (b >> 3);
  const int tid = threadIdx.x, lane = tid & 63, w = __builtin_amdgcn_readfirstlane(tid >> 6);
  const int ch = cg * 4 + (w & 3);
  const int j16 = lane & 15, q = lane >> 4;
  const bool isA = w < 4;
  lptr kb = smem + OFF_K + w * KB_STRIDE;
  u64* gq = (u64*)ws;
  u32* gepoch = (u32*)((char*)ws + 65536);
  const rsrc_t ru = mkrsrc(gu, L * D * 2), rk = mkrsrc(gk, L * D * 2), rv = mkrsrc(gv, L * D * 2), rout = mkrsrc(out, L * D * 2);
  STAMP(0);
  lptr cnt = smem + OFF_MISC;
  u64 pc;
  asm volatile("s_getpc_b64 %0" : "=s"(pc));
  if (tid < 16) lds_w32(cnt + tid * 4, 0);
  asm volatile("s_waitcnt lgkmcnt(0)\n\ts_barrier" ::: "memory");
  auto loadA = [&](u32x4 (&A)[16]) {
    const int m = lane & 3, ep = (lane >> 2) & 3;
    lptr ka = kb + 32 + (-12 + 4 * m + 4 * (8 * q + 7 - ep)) * 2;
#pragma unroll
    for (int c = 0; c < 16; c++) {
      u32x2 lo = lds_tr(ka + c * 256), hi = lds_tr(ka + c * 256 - 32);
      A[c] = u32x4{lo[0], lo[1], hi[0], hi[1]};
    }
  };
  const u32 xa = (u32)(size_t)(smem + OFF_X + w * XB_STRIDE + (64 - 32 + 4 * j16 - 8 * q - 4) * 2);
  const int rot = (lane == 0 ? 63 : q == 0 ? lane + 47 : lane - 16) * 4;
  lptr yw = kb + 32 + (16 * j16 + 4 * q) * 2;
  u32x4 A[16];
  const int tb = tid & 255, wb = w & 3;
  auto load_par = [&]() { return bl32(mkrsrc(shk, D * 8), lane < 8 ? cg * 32 + lane * 4 : OOB) | bl32(mkrsrc(Dskip, D * 2), (lane >> 1) == 4 ? cg * 8 + (lane & 1) * 4 : OOB) |
                  bl32(mkrsrc(ssmD, D * 2), (lane >> 1) == 5 ? cg * 8 + (lane & 1) * 4 : OOB); };
  const u32 xoff = (2 * tb * 256 + cg) * 8;
  u32x2 xu[4][2], xkv[4][2];
  XIn in[4];
  auto load_x = [&](int s) {
    const u32 o = xoff + s * 512 * 2048;
    const int hrow = 512 * s + 128 * wb - 3 + lane;
    in[s].k[0] = bl64(rk, o); in[s].k[1] = bl64(rk, o + 2048);
    in[s].h = bl64(rk, (lane < 3 && hrow >= 0) ? (u32)(hrow * 256 + cg) * 8 : OOB);
    in[s].v[0] = bl64(rv, o); in[s].v[1] = bl64(rv, o + 2048);
    xu[s][0] = bl64(ru, o); xu[s][1] = bl64(ru, o + 2048);
    __builtin_amdgcn_sched_barrier(0);
  };
  auto landed = [&](int s) { asm volatile("" : "+v"(xu[s][1])); __builtin_amdgcn_sched_barrier(0); };
  const bool odd2 = lane & 2;
  const u32 sel0 = odd2 ? 0x01000504u : 0x05040100u, sel1 = odd2 ? 0x03020706u : 0x07060302u;
  lptr xw = smem + OFF_X + (odd2 ? 2 * XB_STRIDE : 0) + (lane & 1) * 2 * XP_STRIDE + 128 + (tb >> 2) * 4;
  u32x4 shw0, shw1; u32x2 dsk, dss;
  auto unpack_par = [&](u32 par) {
#pragma unroll
    for (int e = 0; e < 4; e++) { shw0[e] = __builtin_amdgcn_readlane(par, e); shw1[e] = __builtin_amdgcn_readlane(par, 4 + e); }
#pragma unroll
    for (int e = 0; e < 2; e++) { dsk[e] = __builtin_amdgcn_readlane(par, 8 + e); dss[e] = __builtin_amdgcn_readlane(par, 10 + e); }
  };
  auto proc = [&](int s) {
    process_x(xw + s * 256, sel0, sel1, odd2, xu[s], in[s], shw0, shw1, xkv[s]);
    cnt_signal(cnt + 4 * s, lane);
  };
  lptr yr = smem + OFF_K + 32 + tb * 4;
  const u64* gp = gq + tb;
  u64 gr[4];
  auto ldg = [&](int s) { gr[s] = __hip_atomic_load(gp + s * 256, __ATOMIC_RELAXED, __HIP_MEMORY_SCOPE_AGENT); };
  auto epi = [&](int s, u32 tag) {
    cnt_wait(cnt + 16 + 4 * s, 8);
    epilogue(yr + s * 1024, xoff + s * 512 * 2048, tag, gp + s * 256, gr[s], xu[s], xkv[s], dsk, dss, rout);
  };
  auto retire = [&](u32 tag) {
    if (b == 0 && lane == 0) {
      u32 n = __hip_atomic_fetch_add((LDS u32*)(cnt + 32), 1u, __ATOMIC_RELAXED, __HIP_MEMORY_SCOPE_WORKGROUP);
      if (n == 7) __hip_atomic_store(gepoch, tag, __ATOMIC_RELAXED, __HIP_MEMORY_SCOPE_AGENT);
    }
  };

  if (isA) {
    const rsrc_t r1 = mkrsrc(logAr, D * NM * 2), r2 = mkrsrc(Aim, D * NM * 2), r3 = mkrsrc(Cre, D * NM * 2), r4 = mkrsrc(Cim, D * NM * 2);
    const u32 po = (ch * 64 + lane) * 4;
    u32 pAr = bl32(r1, po), pAi = bl32(r2, po), pCr = bl32(r3, po), pCi = bl32(r4, po);
    u32 pdt = bl16(mkrsrc(logdt, D * 2), ch * 2);
    const u32 par = load_par();
    if (w == 0) {
      __builtin_amdgcn_raw_ptr_buffer_load_lds(mkrsrc((const void*)(pc & ~127ull), CODE_BYTES), (LDS void*)(smem + OFF_PF + 4096), 4,
                                               lane < 8 ? ((b >> 3) * 8 + lane) * 128 : OOB, 0, 0, 0);
    }
    STAMP(10);
    float dt = rb(expf(bf(pdt)));
    f32x2 zr, zi; cplx C;
    zr[0] = -rb(expf(blo(pAr))) * dt; zr[1] = -rb(expf(bhi(pAr))) * dt;
    zi[0] = blo(pAi) * dt; zi[1] = bhi(pAi) * dt;
    C.r[0] = 2.f * blo(pCr) * dt; C.r[1] = 2.f * bhi(pCr) * dt;
    C.i[0] = -2.f * blo(pCi) * dt; C.i[1] = -2.f * bhi(pCi) * dt;
    zr *= 1.44269504088896341f; zi *= 0.15915494309189535f;
    STAMP(11);
    lptr tb = smem + OFF_T + w * TB_STRIDE;
    lptr tw = tb + lane * 8, tr = tb + j16 * TB_ROW + q * 16;
    cplx w1 = cexp_p(zr, zi, 1.f), w16 = cexp_p(zr, zi, 16.f), w256 = cexp_p(zr, zi, 256.f);
    u32x4 G[8];
    {
      cplx g; g.r = f32x2{1.f, 1.f}; g.i = f32x2{0.f, 0.f};
      tput(tw, g); g = w16; tput(tw + TB_ROW, g);
#pragma unroll
      for (int i = 2; i < 16; i++) { g = cmul(g, w16); tput(tw + i * TB_ROW, g); }
      wsync();
#pragma unroll
      for (int s = 0; s < 8; s++) G[s] = lds_r128(tr + s * 64);
      wsync();
    }
    STAMP(12);
    w1 = conj(w1); w256 = conj(w256);
    cplx w2 = csq(w1), w4 = csq(w2), w8 = csq(w4), w512 = csq(w256), w1024 = csq(w512);
    lptr kd = kb + 32;
    if (lane < 8) lds_w32(kb + lane * 4, 0);
    {
      cplx e = C; tput(tw, e);
#pragma unroll
      for (int i = 1; i < 16; i++) { e = cmul(e, w1); tput(tw + i * TB_ROW, e); }
      f32x4 a = {0, 0, 0, 0};
      wsync();
#pragma unroll
      for (int s = 0; s < 8; s++) a = mfma(lds_r128(tr + s * 64), G[s], a);
      wsync();
      lds_w64(kd + (16 * j16 + 4 * q) * 2, u32x2{cvtpk(a[0], a[1]), cvtpk(a[2], a[3])});
    }
    STAMP(13);
    {
      cplx e = cmul(C, w256); tput(tw, e);
#pragma unroll
      for (int i = 1; i < 8; i++) { e = cmul(e, w2); tput(tw + i * TB_ROW, e); }
      e = cmul(C, w512);
#pragma unroll
      for (int i = 0; i < 4; i++) { tput(tw + (8 + i) * TB_ROW, e); tput(tw + (12 + i) * TB_ROW, cmul(e, w256)); if (i < 3) e = cmul(e, w4); }
      f32x4 a = {0, 0, 0, 0};
      wsync();
#pragma unroll
      for (int s = 0; s < 8; s++) a = mfma(lds_r128(tr + s * 64), G[s], a);
      wsync();
      u32 p01 = cvtpk(a[0], a[1]), p23 = cvtpk(a[2], a[3]);
      u32 v00 = perm(p01, p01, 0x01000100), v11 = perm(p01, p01, 0x03020302), v22 = perm(p23, p23, 0x01000100), v33 = perm(p23, p23, 0x03020302);
      const int base = q < 2 ? 256 + 16 * j16 + 8 * q : 512 + 256 * (q - 2) + 16 * j16;
      lds_w128(kd + base * 2, q < 2 ? u32x4{v00, perm(p23, p01, 0x05040302), v22, v33} : u32x4{v00, p01, v11, v22});
      if (q >= 2) lds_w128(kd + base * 2 + 16, u32x4{v22, p23, v33, v33});
      wsync();
      if (q >= 2 && base > 512) lds_w32(kd + (base - 2) * 2, v00); else if (base > 256) lds_w16(kd + (base - 1) * 2, (u16)p01);
      wsync();
    }
    STAMP(14);
    load_x(3);
    {
      cplx e0 = cmul(C, w1024), e1 = cmul(e0, w8);
#pragma unroll
      for (int i = 0; i < 4; i++) { tput(tw + (2 * i) * TB_ROW, e0); tput(tw + (2 * i + 1) * TB_ROW, e1); e0 = cmul(e0, w256); if (i < 3) e1 = cmul(e1, w256); }
      tput(tw + 8 * TB_ROW, e0);
      f32x4 a = {0, 0, 0, 0};
      wsync();
#pragma unroll
      for (int s = 0; s < 8; s++) a = mfma(lds_r128(tr + s * 64), G[s], a);
      wsync();
      u32 p01 = cvtpk(a[0], a[1]), p23 = cvtpk(a[2], a[3]);
      if (q < 2) {
#pragma unroll
        for (int hh = 0; hh < 2; hh++) {
          u32 p = hh ? p23 : p01;
          u32 v00 = perm(p, p, 0x01000100), v11 = perm(p, p, 0x03020302);
          int base = 1024 + 512 * q + 256 * hh + 16 * j16;
          lds_w128(kd + base * 2, u32x4{v00, v00, p, v11});
          lds_w128(kd + base * 2 + 16, u32x4{v11, v11, v11, v11});
        }
      }
      wsync();
      if (q < 2) {
#pragma unroll
        for (int hh = 0; hh < 2; hh++) {
          u32 p = hh ? p23 : p01;
          u32 v00 = perm(p, p, 0x01000100);
          int base = 1024 + 512 * q + 256 * hh + 16 * j16;
          if (base > 1024) lds_w64(kd + (base - 4) * 2, u32x2{v00, v00}); else lds_w32(kd + (base - 2) * 2, v00);
        }
      } else if (lane == 32) {
        u32 v00 = perm(p01, p01, 0x01000100);
        lds_w64(kd + 2044 * 2, u32x2{v00, v00});
      }
      wsync();
    }
    STAMP(15);
    loadA(A);
    STAMP(1);
    u32 tag = 0;
    conv(A, xa, yw, rot, lane, cnt, [&](auto k) {
      constexpr int K = k.value;
      STAMP(2 + K);
      if (K == 1) { unpack_par(par); proc(3); }
      if (K == 4) { tag = __builtin_amdgcn_readfirstlane(*(LDS u32*)(cnt + 36)); ldg(3); }
    });
    STAMP(8);
    epi(3, tag);
    retire(tag);
    STAMP(9);
  } else {
    const rsrc_t rs = mkrsrc(ssmk, L * D * 2), rw = mkrsrc(gatew, D * 2);
    const u32 par = load_par();
    load_x(0);
    const u32 epoch = __hip_atomic_load(gepoch, __ATOMIC_RELAXED, __HIP_MEMORY_SCOPE_AGENT);
    const int grow = b * 8 + wb * 2;
    u32x4 gx[4], gw[2], kk[4];
#pragma unroll
    for (int e = 0; e < 4; e++) kk[e] = bl128(rs, ch * 4096 + (e * 64 + lane) * 16);
#pragma unroll
    for (int e = 0; e < 4; e++) gx[e] = bl128(ru, grow * 2048 + (e * 64 + lane) * 16);
#pragma unroll
    for (int e = 0; e < 2; e++) gw[e] = bl128(rw, (e * 64 + lane) * 16);
    STAMP(10);
    for (int i = tb; i < 32 * 32; i += 256) lds_w32(smem + OFF_X + (i >> 7) * XB_STRIDE + ((i >> 5) & 3) * XP_STRIDE + (i & 31) * 4, 0);
    if (lane < 8) lds_w32(kb + lane * 4, 0);
    const u32 tag = epoch + 1;
    auto gate = [&]() {
      u32 gb[2];
#pragma unroll
      for (int rr = 0; rr < 2; rr++) {
        float s = 0.f;
#pragma unroll
        for (int h = 0; h < 2; h++) {
          u32x4 x = gx[rr * 2 + h], ww = gw[h];
#pragma unroll
          for (int e = 0; e < 4; e++) { u32 p = cvtpk(blo(x[e]) * blo(ww[e]), bhi(x[e]) * bhi(ww[e])); s += blo(p) + bhi(p); }
        }
#pragma unroll
        for (int o = 32; o >= 1; o >>= 1) s += __shfl_xor(s, o);
        float z = rb(s), g = 1.f / (1.f + expf(-z));
        gb[rr] = cvtpk(g, g) & 0xffffu;
      }
      if (lane == 0) __hip_atomic_store(gq + (grow >> 1), ((u64)tag << 32) | (gb[0] | gb[1] << 16), __ATOMIC_RELAXED, __HIP_MEMORY_SCOPE_AGENT);
    };
    STAMP(11);
    landed(0);
    unpack_par(par);
    if (lane == 0) lds_w32(cnt + 36, tag);
    STAMP(12);
    load_x(1);
    STAMP(13);
    proc(0);
    STAMP(14);
#pragma unroll
    for (int e = 0; e < 4; e++) lds_w128(kb + 32 + (e * 64 + lane) * 16, kk[e]);
    STAMP(15);
    wsync();
    loadA(A);
    STAMP(1);
    conv(A, xa, yw, rot, lane, cnt, [&](auto k) {
      constexpr int K = k.value;
      STAMP(2 + K);
      if (K == 0) { landed(1); load_x(2); proc(1); }
      if (K == 1) gate();
      if (K == 2) proc(2);
      if (K == 3) { ldg(0); ldg(1); }
      if (K == 4) { epi(0, tag); ldg(2); }
      if (K == 5) epi(1, tag);
    });
    STAMP(8);
    epi(2, tag);
    retire(tag);
    STAMP(9);
  }
}

extern "C" void kernel_launch(void* const* d_in, const int* in_sizes, int n_in, void* d_out, int out_size, void* d_ws,
                              size_t ws_size, hipStream_t stream) {
  (void)hipFuncSetAttribute((const void*)conv_kernel, hipFuncAttributeMaxDynamicSharedMemorySize, LDS_BYTES);
  conv_kernel<<<256, 512, LDS_BYTES, stream>>>(d_in[0], d_in[1], d_in[2], d_in[3], d_in[4], d_in[5], d_in[6], d_in[8], d_in[9],
                                               d_in[10], d_in[11], d_in[12], d_in[13], d_ws, d_out);
}
```

```cpp
#include <hip/hip_runtime.h>
#include <hip/hip_bf16.h>
#include <type_traits>

typedef unsigned int u32;
typedef unsigned short u16;
typedef unsigned long long u64;
typedef u32 u32x2 __attribute__((ext_vector_type(2)));
typedef u32 u32x4 __attribute__((ext_vector_type(4)));
typedef float f32x2 __attribute__((ext_vector_type(2)));
typedef float f32x4 __attribute__((ext_vector_type(4)));
typedef __bf16 bf16x2 __attribute__((ext_vector_type(2)));
typedef __bf16 bf16x8 __attribute__((ext_vector_type(8)));
typedef short s16x4 __attribute__((ext_vector_type(4)));
#define DI __device__ __forceinline__
#define LDS __attribute__((address_space(3)))

constexpr int L = 2048, D = 1024, NM = 128;
constexpr int KB_STRIDE = 4128;
constexpr int XP_STRIDE = 1184;
constexpr int XB_STRIDE = 4 * XP_STRIDE + 64;
constexpr int TB_ROW = 528;
constexpr int TB_STRIDE = 16 * TB_ROW;
constexpr int OFF_K = 0;
constexpr int OFF_X = OFF_K + 8 * KB_STRIDE;
constexpr int OFF_T = OFF_X + 8 * XB_STRIDE;
constexpr int OFF_MISC = OFF_T + 4 * TB_STRIDE;
constexpr int OFF_PF = OFF_MISC + 64;
constexpr int LDS_BYTES = OFF_PF + 4096 + 256;
constexpr int CODE_BYTES = 32768;
#define PRIO 2
#define X3_AT 1
#define P3_AT 3
#define GATE_AT 0
#define CODEPF 1
#define PREFETCH 0

DI u32 cvtpk(float lo, float hi) { f32x2 v = {lo, hi}; bf16x2 b = __builtin_convertvector(v, bf16x2); return __builtin_bit_cast(u32, b); }
DI float blo(u32 x) { return __uint_as_float(x << 16); }
DI float bhi(u32 x) { return __uint_as_float(x & 0xffff0000u); }
DI float rb(float x) { return blo(cvtpk(x, x)); }
DI float bf(u16 x) { return __uint_as_float((u32)x << 16); }
DI f32x4 mfma(u32x4 a, u32x4 b, f32x4 c) {
  return __builtin_amdgcn_mfma_f32_16x16x32_bf16(__builtin_bit_cast(bf16x8, a), __builtin_bit_cast(bf16x8, b), c, 0, 0, 0);
}
typedef LDS char* lptr;
DI u32x2 lds_r64(lptr p) { return *(LDS u32x2*)p; }
DI u32x4 lds_r128(lptr p) { return *(LDS u32x4*)p; }
DI void lds_w32(lptr p, u32 v) { *(LDS u32*)p = v; }
DI void lds_w16(lptr p, u16 v) { *(LDS u16*)p = v; }
DI void lds_w64(lptr p, u32x2 v) { *(LDS u32x2*)p = v; }
DI void lds_w128(lptr p, u32x4 v) { *(LDS u32x4*)p = v; }
DI u32x2 lds_tr(lptr p) {
  typedef short v4i16_t __attribute__((ext_vector_type(4)));
  return __builtin_bit_cast(u32x2, __builtin_amdgcn_ds_read_tr16_b64_v4i16((LDS v4i16_t*)p));
}
DI void wsync() { __builtin_amdgcn_fence(__ATOMIC_ACQ_REL, "wavefront"); __builtin_amdgcn_wave_barrier(); }
DI u32 perm(u32 hi, u32 lo, u32 sel) { return __builtin_amdgcn_perm(hi, lo, sel); }

#define STAMP(i)
#define STAMP_DUMP()
struct cplx { f32x2 r, i; };
DI cplx cmul(cplx a, cplx b) { cplx c; c.r = a.r * b.r - a.i * b.i; c.i = a.r * b.i + a.i * b.r; return c; }
DI cplx csq(cplx a) { cplx c; c.r = a.r * a.r - a.i * a.i; f32x2 t = a.r * a.i; c.i = t + t; return c; }
DI cplx conj(cplx a) { cplx c; c.r = a.r; c.i = -a.i; return c; }
DI cplx cexp_p(f32x2 zr2, f32x2 zi_rev, float p) {
  cplx c;
#pragma unroll
  for (int m = 0; m < 2; m++) {
    float mag = __builtin_amdgcn_exp2f(zr2[m] * p);
    float ph = __builtin_amdgcn_fractf(zi_rev[m] * p);
    c.r[m] = mag * __builtin_amdgcn_cosf(ph);
    c.i[m] = mag * __builtin_amdgcn_sinf(ph);
  }
  return c;
}
DI void tput(lptr trow, cplx e) { u32x2 v; v[0] = cvtpk(e.r[0], e.i[0]); v[1] = cvtpk(e.r[1], e.i[1]); lds_w64(trow, v); }

typedef __amdgpu_buffer_rsrc_t rsrc_t;
DI rsrc_t mkrsrc(const void* p, u32 bytes) { return __builtin_amdgcn_make_buffer_rsrc((void*)p, 0, bytes, 0x00020000); }
DI u32 bl16(rsrc_t r, u32 off) { return __builtin_amdgcn_raw_buffer_load_b16(r, off, 0, 0); }
DI u32 bl32(rsrc_t r, u32 off) { return __builtin_amdgcn_raw_buffer_load_b32(r, off, 0, 0); }
DI u32x2 bl64(rsrc_t r, u32 off) { return __builtin_bit_cast(u32x2, __builtin_amdgcn_raw_buffer_load_b64(r, off, 0, 0)); }
DI u32x4 bl128(rsrc_t r, u32 off) { return __builtin_bit_cast(u32x4, __builtin_amdgcn_raw_buffer_load_b128(r, off, 0, 0)); }
constexpr u32 OOB = 0x80000000u;

template <int OFF> DI void rdB(u32x2& lo, u32x2& hi, u32 a) {
  asm volatile("ds_read_b64 %0, %2 offset:%3\n\tds_read_b64 %1, %2 offset:%4" : "=&v"(lo), "=&v"(hi) : "v"(a), "i"(OFF), "i"(OFF + 8) : "memory");
}
struct BF { u32x2 lo[4], hi[4]; };
template <int SG> DI void loadB(BF& b, u32 xa) {
  rdB<0 * XP_STRIDE + SG * 64>(b.lo[0], b.hi[0], xa);
  rdB<1 * XP_STRIDE + SG * 64>(b.lo[1], b.hi[1], xa);
  rdB<2 * XP_STRIDE + SG * 64>(b.lo[2], b.hi[2], xa);
  rdB<3 * XP_STRIDE + SG * 64>(b.lo[3], b.hi[3], xa);
}
DI void waitB(BF& b) {
  asm volatile("s_waitcnt lgkmcnt(0)" : "+v"(b.lo[0]), "+v"(b.hi[0]), "+v"(b.lo[1]), "+v"(b.hi[1]), "+v"(b.lo[2]), "+v"(b.hi[2]), "+v"(b.lo[3]), "+v"(b.hi[3]) :: "memory");
}
typedef f32x4 Acc[2][4][2];
template <int SG, int P0, int P1> DI void groupMM(const u32x4 (&A)[16], const BF& b, Acc& acc) {
#pragma unroll
  for (int r = 0; r < 4; r++) {
    u32x4 B = {b.lo[r][0], b.lo[r][1], b.hi[r][0], b.hi[r][1]};
#pragma unroll
    for (int sl = 0; sl < 2; sl++) {
      const int P = sl ? P1 : P0;
      if (P < 0) continue;
#pragma unroll
      for (int hh = 0; hh < 2; hh++) {
        const int c = 2 * (2 * P + hh) - (SG - 1);
        if (c >= 0) acc[sl][r][hh] = mfma(A[c], B, SG == 0 ? f32x4{0, 0, 0, 0} : acc[sl][r][hh]);
      }
    }
  }
}
template <int S, int P0, int P1, bool PRE, int NEXT> DI void block(const u32x4 (&A)[16], Acc& acc, BF& b0, u32 xa) {
  BF b1;
  if (!PRE) { loadB<4 * S>(b0, xa); waitB(b0); }
  loadB<4 * S + 1>(b1, xa);
  __builtin_amdgcn_sched_barrier(0);
  groupMM<4 * S, P0, P1>(A, b0, acc);
  __builtin_amdgcn_sched_barrier(0);
  waitB(b1);
  loadB<4 * S + 2>(b0, xa);
  __builtin_amdgcn_sched_barrier(0);
  groupMM<4 * S + 1, P0, P1>(A, b1, acc);
  __builtin_amdgcn_sched_barrier(0);
  waitB(b0);
  loadB<4 * S + 3>(b1, xa);
  __builtin_amdgcn_sched_barrier(0);
  groupMM<4 * S + 2, P0, P1>(A, b0, acc);
  __builtin_amdgcn_sched_barrier(0);
  waitB(b1);
  if (NEXT >= 0) loadB<(NEXT >= 0 ? NEXT : 0)>(b0, xa);
  __builtin_amdgcn_sched_barrier(0);
  groupMM<4 * S + 3, P0, P1>(A, b1, acc);
  __builtin_amdgcn_sched_barrier(0);
  if (NEXT >= 0) waitB(b0);
}
template <int P> DI void finish(const f32x4 (&a)[4][2], float (&carry)[3], lptr yw, int rot, bool l0) {
  float t[2][3];
#pragma unroll
  for (int hh = 0; hh < 2; hh++) {
    float c0 = a[1][hh][3] + a[2][hh][2] + a[3][hh][1], c1 = a[2][hh][3] + a[3][hh][2], c2 = a[3][hh][3];
    t[hh][0] = __int_as_float(__builtin_amdgcn_ds_bpermute(rot, __float_as_int(c0)));
    t[hh][1] = __int_as_float(__builtin_amdgcn_ds_bpermute(rot, __float_as_int(c1)));
    t[hh][2] = __int_as_float(__builtin_amdgcn_ds_bpermute(rot, __float_as_int(c2)));
  }
#pragma unroll
  for (int hh = 0; hh < 2; hh++) {
    float x[3];
#pragma unroll
    for (int k = 0; k < 3; k++) x[k] = l0 ? (hh ? t[0][k] : (P ? carry[k] : 0.f)) : t[hh][k];
    float y0 = a[0][hh][0] + x[0], y1 = a[0][hh][1] + a[1][hh][0] + x[1];
    float y2 = a[0][hh][2] + a[1][hh][1] + a[2][hh][0] + x[2], y3 = a[0][hh][3] + a[1][hh][2] + a[2][hh][1] + a[3][hh][0];
    lds_w64(yw + 512 * (2 * P + hh), u32x2{cvtpk(y0, y1), cvtpk(y2, y3)});
  }
#pragma unroll
  for (int k = 0; k < 3; k++) carry[k] = t[1][k];
}

DI void cnt_signal(lptr p, int lane) {
  asm volatile("s_waitcnt lgkmcnt(0)" ::: "memory");
  if (lane == 0) __hip_atomic_fetch_add((LDS u32*)p, 1u, __ATOMIC_RELAXED, __HIP_MEMORY_SCOPE_WORKGROUP);
  asm volatile("" ::: "memory");
}
DI void cnt_wait(lptr p, u32 target) {
  asm volatile("" ::: "memory");
#pragma unroll 1
  for (int it = 0; it < (1 << 22); it++) {
    u32 v = __builtin_amdgcn_readfirstlane(__hip_atomic_load((LDS u32*)p, __ATOMIC_RELAXED, __HIP_MEMORY_SCOPE_WORKGROUP));
    if (v >= target) break;
    __builtin_amdgcn_s_sleep(1);
  }
  asm volatile("" ::: "memory");
}

struct XIn { u32x2 k[2], v[2], h; };
DI void process_x(lptr xw, u32 sel0, u32 sel1, bool odd2, const u32x2 (&xu)[2], const XIn& in, u32x4 shw0, u32x4 shw1,
                  u32x2 (&xkv)[2]) {
  u32x2 kr[5];
#pragma unroll
  for (int d = 0; d < 2; d++) {
    u32 h1 = __builtin_amdgcn_update_dpp(0u, in.h[d], 0x101, 0xf, 0xf, true);
    u32 h2 = __builtin_amdgcn_update_dpp(0u, in.h[d], 0x102, 0xf, 0xf, true);
    kr[1][d] = __builtin_amdgcn_update_dpp(h1, in.k[0][d], 0x138, 0xf, 0xf, false);
    kr[2][d] = __builtin_amdgcn_update_dpp(h2, in.k[1][d], 0x138, 0xf, 0xf, false);
    kr[0][d] = __builtin_amdgcn_update_dpp(in.h[d], kr[2][d], 0x138, 0xf, 0xf, false);
    kr[3][d] = in.k[0][d]; kr[4][d] = in.k[1][d];
  }
#pragma unroll
  for (int cp = 0; cp < 2; cp++) {
    u32x4 sw = cp ? shw1 : shw0;
    f32x2 t0 = {blo(sw[0]), blo(sw[2])}, t1 = {bhi(sw[0]), bhi(sw[2])}, t2 = {blo(sw[1]), blo(sw[3])}, t3 = {bhi(sw[1]), bhi(sw[3])};
    f32x2 kf[5];
#pragma unroll
    for (int e = 0; e < 5; e++) kf[e] = f32x2{blo(kr[e][cp]), bhi(kr[e][cp])};
#pragma unroll
    for (int e = 0; e < 2; e++) {
      f32x2 p3 = kf[e] * t3, p2 = kf[e + 1] * t2, p1 = kf[e + 2] * t1, p0 = kf[e + 3] * t0;
      u32 u3 = cvtpk(p3[0], p3[1]), u2 = cvtpk(p2[0], p2[1]), u1 = cvtpk(p1[0], p1[1]), u0 = cvtpk(p0[0], p0[1]);
      f32x2 s = f32x2{blo(u3), bhi(u3)} + f32x2{blo(u2), bhi(u2)};
      u32 us = cvtpk(s[0], s[1]);
      s = f32x2{blo(us), bhi(us)} + f32x2{blo(u1), bhi(u1)}; us = cvtpk(s[0], s[1]);
      s = f32x2{blo(us), bhi(us)} + f32x2{blo(u0), bhi(u0)}; us = cvtpk(s[0], s[1]);
      s = f32x2{blo(us), bhi(us)} * f32x2{blo(in.v[e][cp]), bhi(in.v[e][cp])};
      xkv[e][cp] = cvtpk(s[0], s[1]);
    }
  }
#pragma unroll
  for (int arr = 0; arr < 2; arr++) {
#pragma unroll
    for (int a = 0; a < 2; a++) {
      u32x2 x = arr ? xkv[a] : xu[a];
      u32 keep = odd2 ? x[1] : x[0], send = odd2 ? x[0] : x[1];
      u32 other = __builtin_amdgcn_update_dpp(0u, send, 0x4e, 0xf, 0xf, true);
      lds_w32(xw + arr * 4 * XB_STRIDE + a * XP_STRIDE, perm(other, keep, sel0));
      lds_w32(xw + (arr * 4 + 1) * XB_STRIDE + a * XP_STRIDE, perm(other, keep, sel1));
    }
  }
}

DI void epilogue(lptr yr, u32 ooff, u32 tag, const u64* gp, u64 gr, const u32x2 (&xu)[2], const u32x2 (&xkv)[2], u32x2 dsk,
                 u32x2 dss, rsrc_t rout) {
  u32 ya[4], yb[4];
#pragma unroll
  for (int c = 0; c < 4; c++) {
    ya[c] = *(LDS u32*)(yr + c * KB_STRIDE);
    yb[c] = *(LDS u32*)(yr + (4 + c) * KB_STRIDE);
  }
  if (!__all((u32)(gr >> 32) == tag)) {
#pragma unroll 1
    for (int it = 0; it < (1 << 16); it++) {
      __builtin_amdgcn_s_sleep(2);
      gr = __hip_atomic_load(gp, __ATOMIC_RELAXED, __HIP_MEMORY_SCOPE_AGENT);
      if (__all((u32)(gr >> 32) == tag)) break;
    }
  }
#pragma unroll
  for (int e = 0; e < 2; e++) {
    float g = e ? bhi((u32)gr) : blo((u32)gr), g1 = rb(1.f - g);
    u32x2 o;
#pragma unroll
    for (int cp = 0; cp < 2; cp++) {
      float res[2];
#pragma unroll
      for (int hl = 0; hl < 2; hl++) {
        int c = cp * 2 + hl;
        float uu = hl ? bhi(xu[e][cp]) : blo(xu[e][cp]);
        float kv = hl ? bhi(xkv[e][cp]) : blo(xkv[e][cp]);
        float d1 = hl ? bhi(dsk[cp]) : blo(dsk[cp]);
        float d2 = hl ? bhi(dss[cp]) : blo(dss[cp]);
        float y1 = e ? bhi(ya[c]) : blo(ya[c]);
        float y2 = e ? bhi(yb[c]) : blo(yb[c]);
        float a = rb(y1 + rb(uu * d1));
        float bb = rb(uu * rb(y2 + rb(kv * d2)));
        res[hl] = rb(g * a) + rb(g1 * bb);
      }
      o[cp] = cvtpk(res[0], res[1]);
    }
    __builtin_amdgcn_raw_buffer_store_b64(__builtin_bit_cast(__attribute__((ext_vector_type(2))) unsigned, o), rout, ooff + e * 2048, 0, 0);
  }
}

template <class F> DI void conv(const u32x4 (&A)[16], u32 xa, lptr yw, int rot, int lane, lptr cnt, F hook) {
  Acc acc; BF b0; float carry[3];
  const bool l0 = lane == 0;
  cnt_wait(cnt + 0, 4);
  block<0, 0, 1, false, 0>(A, acc, b0, xa);
  finish<0>(acc[0], carry, yw, rot, l0);
  cnt_signal(cnt + 16, lane);
  hook(std::integral_constant<int, 0>{});
  block<0, 2, -1, true, -1>(A, acc, b0, xa);
  hook(std::integral_constant<int, 1>{});
  cnt_wait(cnt + 4, 4);
  block<1, 2, 1, false, 0>(A, acc, b0, xa);
  finish<1>(acc[1], carry, yw, rot, l0);
  cnt_signal(cnt + 20, lane);
  hook(std::integral_constant<int, 2>{});
  block<0, -1, 3, true, 4>(A, acc, b0, xa);
  hook(std::integral_constant<int, 3>{});
  block<1, -1, 3, true, -1>(A, acc, b0, xa);
  hook(std::integral_constant<int, 4>{});
  cnt_wait(cnt + 8, 4);
  block<2, 2, 3, false, -1>(A, acc, b0, xa);
  finish<2>(acc[0], carry, yw, rot, l0);
  cnt_signal(cnt + 24, lane);
  hook(std::integral_constant<int, 5>{});
  cnt_wait(cnt + 12, 4);
  block<3, -1, 3, false, -1>(A, acc, b0, xa);
  finish<3>(acc[1], carry, yw, rot, l0);
  cnt_signal(cnt + 28, lane);
}

__global__ __launch_bounds__(512, 2) void conv_kernel(
    const void* __restrict__ gu, const void* __restrict__ logAr, const void* __restrict__ Aim, const void* __restrict__ Cre,
    const void* __restrict__ Cim, const void* __restrict__ logdt, const void* __restrict__ Dskip, const void* __restrict__ gk,
    const void* __restrict__ gv, const void* __restrict__ shk, const void* __restrict__ ssmk, const void* __restrict__ ssmD,
    const void* __restrict__ gatew, void* ws, void* __restrict__ out) {
  extern __shared__ __attribute__((aligned(16))) char smem_[];
  lptr smem = (lptr)smem_;
  const int b = blockIdx.x, cg = (b & 7) * 32 + (b >> 3);
  const int tid = threadIdx.x, lane = tid & 63, w = __builtin_amdgcn_readfirstlane(tid >> 6);
  const int ch = cg * 4 + (w & 3);
  const int j16 = lane & 15, q = lane >> 4;
  const bool isA = w < 4;
  lptr kb = smem + OFF_K + w * KB_STRIDE;
  u64* gq = (u64*)ws;
  u32* gepoch = (u32*)((char*)ws + 65536);
  const rsrc_t ru = mkrsrc(gu, L * D * 2), rk = mkrsrc(gk, L * D * 2), rv = mkrsrc(gv, L * D * 2), rout = mkrsrc(out, L * D * 2);
  STAMP(0);
  lptr cnt = smem + OFF_MISC;
  u64 pc;
  asm volatile("s_getpc_b64 %0" : "=s"(pc));
  if (tid < 16) lds_w32(cnt + tid * 4, 0);
  asm volatile("s_waitcnt lgkmcnt(0)\n\ts_barrier" ::: "memory");
  auto loadA = [&](u32x4 (&A)[16]) {
    const int m = lane & 3, ep = (lane >> 2) & 3;
    lptr ka = kb + 32 + (-12 + 4 * m + 4 * (8 * q + 7 - ep)) * 2;
#pragma unroll
    for (int c = 0; c < 16; c++) {
      u32x2 lo = lds_tr(ka + c * 256), hi = lds_tr(ka + c * 256 - 32);
      A[c] = u32x4{lo[0], lo[1], hi[0], hi[1]};
    }
  };
  const u32 xa = (u32)(size_t)(smem + OFF_X + w * XB_STRIDE + (64 - 32 + 4 * j16 - 8 * q - 4) * 2);
  const int rot = (lane == 0 ? 63 : q == 0 ? lane + 47 : lane - 16) * 4;
  lptr yw = kb + 32 + (16 * j16 + 4 * q) * 2;
  u32x4 A[16];
  const int tb = tid & 255, wb = w & 3;
  const u32 xoff = (2 * tb * 256 + cg) * 8;
  u32x2 xu[4][2], xkv[4][2];
  XIn in[4];
  auto load_x = [&](int s) {
    const u32 o = xoff + s * 512 * 2048;
    const int hrow = 512 * s + 128 * wb - 3 + lane;
    in[s].k[0] = bl64(rk, o); in[s].k[1] = bl64(rk, o + 2048);
    in[s].h = bl64(rk, (lane < 3 && hrow >= 0) ? (u32)(hrow * 256 + cg) * 8 : OOB);
    in[s].v[0] = bl64(rv, o); in[s].v[1] = bl64(rv, o + 2048);
    xu[s][0] = bl64(ru, o); xu[s][1] = bl64(ru, o + 2048);
    __builtin_amdgcn_sched_barrier(0);
  };
  auto landed = [&](int s) { asm volatile("" : "+v"(xu[s][1])); __builtin_amdgcn_sched_barrier(0); };
  const bool odd2 = lane & 2;
  const u32 sel0 = odd2 ? 0x01000504u : 0x05040100u, sel1 = odd2 ? 0x03020706u : 0x07060302u;
  lptr xw = smem + OFF_X + (odd2 ? 2 * XB_STRIDE : 0) + (lane & 1) * 2 * XP_STRIDE + 128 + (tb >> 2) * 4;
  u32x4 shw0, shw1; u32x2 dsk, dss;
  auto load_par = [&]() {
    const u32* p1 = (const u32*)shk + cg * 8;
#pragma unroll
    for (int e = 0; e < 4; e++) { shw0[e] = p1[e]; shw1[e] = p1[4 + e]; }
#pragma unroll
    for (int e = 0; e < 2; e++) { dsk[e] = ((const u32*)Dskip)[cg * 2 + e]; dss[e] = ((const u32*)ssmD)[cg * 2 + e]; }
  };
  auto proc = [&](int s) {
    process_x(xw + s * 256, sel0, sel1, odd2, xu[s], in[s], shw0, shw1, xkv[s]);
    cnt_signal(cnt + 4 * s, lane);
  };
  lptr yr = smem + OFF_K + 32 + tb * 4;
  const u64* gp = gq + tb;
  u64 gr[4];
  auto ldg = [&](int s) { gr[s] = __hip_atomic_load(gp + s * 256, __ATOMIC_RELAXED, __HIP_MEMORY_SCOPE_AGENT); };
  auto epi = [&](int s, u32 tag) {
    cnt_wait(cnt + 16 + 4 * s, 8);
    epilogue(yr + s * 1024, xoff + s * 512 * 2048, tag, gp + s * 256, gr[s], xu[s], xkv[s], dsk, dss, rout);
  };
  auto retire = [&](u32 tag) {
    if (b == 0 && lane == 0) {
      u32 n = __hip_atomic_fetch_add((LDS u32*)(cnt + 32), 1u, __ATOMIC_RELAXED, __HIP_MEMORY_SCOPE_WORKGROUP);
      if (n == 7) __hip_atomic_store(gepoch, tag, __ATOMIC_RELAXED, __HIP_MEMORY_SCOPE_AGENT);
    }
  };

  if (isA) {
    const rsrc_t r1 = mkrsrc(logAr, D * NM * 2), r2 = mkrsrc(Aim, D * NM * 2), r3 = mkrsrc(Cre, D * NM * 2), r4 = mkrsrc(Cim, D * NM * 2);
    const u32 po = (ch * 64 + lane) * 4;
    u32 pAr = bl32(r1, po), pAi = bl32(r2, po), pCr = bl32(r3, po), pCi = bl32(r4, po);
    const u32 pdt = (((const u32*)logdt)[ch >> 1] >> ((ch & 1) * 16)) & 0xffffu;
    load_par();
    const rsrc_t rw = mkrsrc(gatew, D * 2);
    const int grow = b * 8 + wb * 2;
    u32x4 gx[4], gw[2];
#pragma unroll
    for (int e = 0; e < 4; e++) gx[e] = bl128(ru, grow * 2048 + (e * 64 + lane) * 16);
#pragma unroll
    for (int e = 0; e < 2; e++) gw[e] = bl128(rw, (e * 64 + lane) * 16);
    const u32 tag = __hip_atomic_load(gepoch, __ATOMIC_RELAXED, __HIP_MEMORY_SCOPE_AGENT) + 1;
    __builtin_amdgcn_sched_barrier(0);
    auto gate = [&]() {
      u32 gb[2];
#pragma unroll
      for (int rr = 0; rr < 2; rr++) {
        float s = 0.f;
#pragma unroll
        for (int h = 0; h < 2; h++) {
          u32x4 x = gx[rr * 2 + h], ww = gw[h];
#pragma unroll
          for (int e = 0; e < 4; e++) { u32 p = cvtpk(blo(x[e]) * blo(ww[e]), bhi(x[e]) * bhi(ww[e])); s += blo(p) + bhi(p); }
        }
#pragma unroll
        for (int o = 32; o >= 1; o >>= 1) s += __shfl_xor(s, o);
        float z = rb(s), g = 1.f / (1.f + expf(-z));
        gb[rr] = cvtpk(g, g) & 0xffffu;
      }
      if (lane == 0) __hip_atomic_store(gq + (grow >> 1), ((u64)tag << 32) | (gb[0] | gb[1] << 16), __ATOMIC_RELAXED, __HIP_MEMORY_SCOPE_AGENT);
    };
    if (w == 0) {
      __builtin_amdgcn_raw_ptr_buffer_load_lds(mkrsrc((const void*)(pc & ~127ull), CODE_BYTES), (LDS void*)(smem + OFF_PF + 4096), 4,
                                               lane < 8 ? ((b >> 3) * 8 + lane) * 128 : OOB, 0, 0, 0);
    }
    STAMP(10);
    if (PRIO & 1) __builtin_amdgcn_s_setprio(1);
    float dt = rb(expf(bf(pdt)));
    f32x2 zr, zi; cplx C;
    zr[0] = -rb(expf(blo(pAr))) * dt; zr[1] = -rb(expf(bhi(pAr))) * dt;
    zi[0] = blo(pAi) * dt; zi[1] = bhi(pAi) * dt;
    C.r[0] = 2.f * blo(pCr) * dt; C.r[1] = 2.f * bhi(pCr) * dt;
    C.i[0] = -2.f * blo(pCi) * dt; C.i[1] = -2.f * bhi(pCi) * dt;
    zr *= 1.44269504088896341f; zi *= 0.15915494309189535f;
    STAMP(11);
    lptr tb = smem + OFF_T + w * TB_STRIDE;
    lptr tw = tb + lane * 8, tr = tb + j16 * TB_ROW + q * 16;
    cplx w1 = cexp_p(zr, zi, 1.f), w16 = cexp_p(zr, zi, 16.f), w256 = cexp_p(zr, zi, 256.f);
    u32x4 G[8];
    {
      cplx g; g.r = f32x2{1.f, 1.f}; g.i = f32x2{0.f, 0.f};
      tput(tw, g); g = w16; tput(tw + TB_ROW, g);
#pragma unroll
      for (int i = 2; i < 16; i++) { g = cmul(g, w16); tput(tw + i * TB_ROW, g); }
      wsync();
#pragma unroll
      for (int s = 0; s < 8; s++) G[s] = lds_r128(tr + s * 64);
      wsync();
    }
    STAMP(12);
    w1 = conj(w1); w256 = conj(w256);
    cplx w2 = csq(w1), w4 = csq(w2), w8 = csq(w4), w512 = csq(w256), w1024 = csq(w512);
    lptr kd = kb + 32;
    if (lane < 8) lds_w32(kb + lane * 4, 0);
    {
      cplx e = C; tput(tw, e);
#pragma unroll
      for (int i = 1; i < 16; i++) { e = cmul(e, w1); tput(tw + i * TB_ROW, e); }
      f32x4 a = {0, 0, 0, 0};
      wsync();
#pragma unroll
      for (int s = 0; s < 8; s++) a = mfma(lds_r128(tr + s * 64), G[s], a);
      wsync();
      lds_w64(kd + (16 * j16 + 4 * q) * 2, u32x2{cvtpk(a[0], a[1]), cvtpk(a[2], a[3])});
    }
    STAMP(13);
    {
      cplx e = cmul(C, w256); tput(tw, e);
#pragma unroll
      for (int i = 1; i < 8; i++) { e = cmul(e, w2); tput(tw + i * TB_ROW, e); }
      e = cmul(C, w512);
#pragma unroll
      for (int i = 0; i < 4; i++) { tput(tw + (8 + i) * TB_ROW, e); tput(tw + (12 + i) * TB_ROW, cmul(e, w256)); if (i < 3) e = cmul(e, w4); }
      f32x4 a = {0, 0, 0, 0};
      wsync();
#pragma unroll
      for (int s = 0; s < 8; s++) a = mfma(lds_r128(tr + s * 64), G[s], a);
      wsync();
      u32 p01 = cvtpk(a[0], a[1]), p23 = cvtpk(a[2], a[3]);
      u32 v00 = perm(p01, p01, 0x01000100), v11 = perm(p01, p01, 0x03020302), v22 = perm(p23, p23, 0x01000100), v33 = perm(p23, p23, 0x03020302);
      const int base = q < 2 ? 256 + 16 * j16 + 8 * q : 512 + 256 * (q - 2) + 16 * j16;
      lds_w128(kd + base * 2, q < 2 ? u32x4{v00, perm(p23, p01, 0x05040302), v22, v33} : u32x4{v00, p01, v11, v22});
      if (q >= 2) lds_w128(kd + base * 2 + 16, u32x4{v22, p23, v33, v33});
      wsync();
      if (q >= 2 && base > 512) lds_w32(kd + (base - 2) * 2, v00); else if (base > 256) lds_w16(kd + (base - 1) * 2, (u16)p01);
      wsync();
    }
    STAMP(14);
    {
      cplx e0 = cmul(C, w1024), e1 = cmul(e0, w8);
#pragma unroll
      for (int i = 0; i < 4; i++) { tput(tw + (2 * i) * TB_ROW, e0); tput(tw + (2 * i + 1) * TB_ROW, e1); e0 = cmul(e0, w256); if (i < 3) e1 = cmul(e1, w256); }
      tput(tw + 8 * TB_ROW, e0);
      f32x4 a = {0, 0, 0, 0};
      wsync();
#pragma unroll
      for (int s = 0; s < 8; s++) a = mfma(lds_r128(tr + s * 64), G[s], a);
      wsync();
      u32 p01 = cvtpk(a[0], a[1]), p23 = cvtpk(a[2], a[3]);
      if (q < 2) {
#pragma unroll
        for (int hh = 0; hh < 2; hh++) {
          u32 p = hh ? p23 : p01;
          u32 v00 = perm(p, p, 0x01000100), v11 = perm(p, p, 0x03020302);
          int base = 1024 + 512 * q + 256 * hh + 16 * j16;
          lds_w128(kd + base * 2, u32x4{v00, v00, p, v11});
          lds_w128(kd + base * 2 + 16, u32x4{v11, v11, v11, v11});
        }
      }
      wsync();
      if (q < 2) {
#pragma unroll
        for (int hh = 0; hh < 2; hh++) {
          u32 p = hh ? p23 : p01;
          u32 v00 = perm(p, p, 0x01000100);
          int base = 1024 + 512 * q + 256 * hh + 16 * j16;
          if (base > 1024) lds_w64(kd + (base - 4) * 2, u32x2{v00, v00}); else lds_w32(kd + (base - 2) * 2, v00);
        }
      } else if (lane == 32) {
        u32 v00 = perm(p01, p01, 0x01000100);
        lds_w64(kd + 2044 * 2, u32x2{v00, v00});
      }
      wsync();
    }
    STAMP(15);
    if (PRIO & 1) __builtin_amdgcn_s_setprio(0);
    if (lane == 0) lds_w32(cnt + 36, tag);
    loadA(A);
    STAMP(1);
    conv(A, xa, yw, rot, lane, cnt, [&](auto k) {
      constexpr int K = k.value;
      STAMP(2 + K);
      if (PRIO & 8) __builtin_amdgcn_s_setprio(1);
      if (K == GATE_AT) gate();
      if (K == X3_AT) load_x(3);
      if (K == P3_AT) proc(3);
      if (K == 4) ldg(3);
      if (PRIO & 8) __builtin_amdgcn_s_setprio(0);
    });
    STAMP(8);
    epi(3, tag);
    retire(tag);
    STAMP(9);
    STAMP_DUMP();
  } else {
    const rsrc_t rs = mkrsrc(ssmk, L * D * 2);
    load_x(0);
    u32x4 kk[4];
#pragma unroll
    for (int e = 0; e < 4; e++) kk[e] = bl128(rs, ch * 4096 + (e * 64 + lane) * 16);
    load_par();
    __builtin_amdgcn_sched_barrier(0);
    STAMP(10);
    for (int i = tb; i < 32 * 32; i += 256) lds_w32(smem + OFF_X + (i >> 7) * XB_STRIDE + ((i >> 5) & 3) * XP_STRIDE + (i & 31) * 4, 0);
    if (lane < 8) lds_w32(kb + lane * 4, 0);
    STAMP(11);
    landed(0);
    STAMP(12);
    load_x(1);
    proc(0);
    STAMP(13);
#pragma unroll
    for (int e = 0; e < 4; e++) lds_w128(kb + 32 + (e * 64 + lane) * 16, kk[e]);
    wsync();
    STAMP(14);
    STAMP(15);
    loadA(A);
    STAMP(1);
    u32 tag = 0;
    if (PRIO & 2) __builtin_amdgcn_s_setprio(1);
    conv(A, xa, yw, rot, lane, cnt, [&](auto k) {
      constexpr int K = k.value;
      STAMP(2 + K);
      if (PRIO & 4) __builtin_amdgcn_s_setprio(1);
      if (K == 0) { landed(1); load_x(2); proc(1); }
      if (K == 2) proc(2);
      if (K == 3) { ldg(0); ldg(1); ldg(2); }
      if (K == 4) {
        cnt_wait(cnt + 16, 8);
        tag = __builtin_amdgcn_readfirstlane(*(LDS u32*)(cnt + 36));
        epi(0, tag);
      }
      if (K == 5) epi(1, tag);
      if (PRIO & 4) __builtin_amdgcn_s_setprio(0);
    });
    STAMP(8);
    epi(2, tag);
    retire(tag);
    STAMP(9);
    STAMP_DUMP();
  }
}

extern "C" void kernel_launch(void* const* d_in, const int* in_sizes, int n_in, void* d_out, int out_size, void* d_ws,
                              size_t ws_size, hipStream_t stream) {
  (void)hipFuncSetAttribute((const void*)conv_kernel, hipFuncAttributeMaxDynamicSharedMemorySize, LDS_BYTES);
  conv_kernel<<<256, 512, LDS_BYTES, stream>>>(d_in[0], d_in[1], d_in[2], d_in[3], d_in[4], d_in[5], d_in[6], d_in[8], d_in[9],
                                               d_in[10], d_in[11], d_in[12], d_in[13], d_ws, d_out);
}
```

```cpp
#include <hip/hip_runtime.h>
#include <hip/hip_bf16.h>
#include <type_traits>

typedef unsigned int u32;
typedef unsigned short u16;
typedef unsigned long long u64;
typedef u32 u32x2 __attribute__((ext_vector_type(2)));
typedef u32 u32x4 __attribute__((ext_vector_type(4)));
typedef float f32x2 __attribute__((ext_vector_type(2)));
typedef float f32x4 __attribute__((ext_vector_type(4)));
typedef __bf16 bf16x2 __attribute__((ext_vector_type(2)));
typedef __bf16 bf16x8 __attribute__((ext_vector_type(8)));
typedef short s16x4 __attribute__((ext_vector_type(4)));
#define DI __device__ __forceinline__
#define LDS __attribute__((address_space(3)))

constexpr int L = 2048, D = 1024, NM = 128;
constexpr int KB_STRIDE = 4128;
constexpr int XP_STRIDE = 1184;
constexpr int XB_STRIDE = 4 * XP_STRIDE + 64;
constexpr int TB_ROW = 528;
constexpr int TB_STRIDE = 16 * TB_ROW;
constexpr int OFF_K = 0;
constexpr int OFF_X = OFF_K + 8 * KB_STRIDE;
constexpr int OFF_T = OFF_X + 8 * XB_STRIDE;
constexpr int OFF_MISC = OFF_T + 4 * TB_STRIDE;
constexpr int OFF_PF = OFF_MISC + 64;
constexpr int LDS_BYTES = OFF_PF + 4096 + 256;
constexpr int CODE_BYTES = 32768;
#define PRIO 6
#define X3_AT 1
#define P3_AT 3
#define GATE_AT 0
#define CODEPF 1
#define PREFETCH 1

DI u32 cvtpk(float lo, float hi) { f32x2 v = {lo, hi}; bf16x2 b = __builtin_convertvector(v, bf16x2); return __builtin_bit_cast(u32, b); }
DI float blo(u32 x) { return __uint_as_float(x << 16); }
DI float bhi(u32 x) { return __uint_as_float(x & 0xffff0000u); }
DI float rb(float x) { return blo(cvtpk(x, x)); }
DI float bf(u16 x) { return __uint_as_float((u32)x << 16); }
DI f32x4 mfma(u32x4 a, u32x4 b, f32x4 c) {
  return __builtin_amdgcn_mfma_f32_16x16x32_bf16(__builtin_bit_cast(bf16x8, a), __builtin_bit_cast(bf16x8, b), c, 0, 0, 0);
}
typedef LDS char* lptr;
DI u32x2 lds_r64(lptr p) { return *(LDS u32x2*)p; }
DI u32x4 lds_r128(lptr p) { return *(LDS u32x4*)p; }
DI void lds_w32(lptr p, u32 v) { *(LDS u32*)p = v; }
DI void lds_w16(lptr p, u16 v) { *(LDS u16*)p = v; }
DI void lds_w64(lptr p, u32x2 v) { *(LDS u32x2*)p = v; }
DI void lds_w128(lptr p, u32x4 v) { *(LDS u32x4*)p = v; }
DI u32x2 lds_tr(lptr p) {
  typedef short v4i16_t __attribute__((ext_vector_type(4)));
  return __builtin_bit_cast(u32x2, __builtin_amdgcn_ds_read_tr16_b64_v4i16((LDS v4i16_t*)p));
}
DI void wsync() { __builtin_amdgcn_fence(__ATOMIC_ACQ_REL, "wavefront"); __builtin_amdgcn_wave_barrier(); }
DI u32 perm(u32 hi, u32 lo, u32 sel) { return __builtin_amdgcn_perm(hi, lo, sel); }

#define STAMP(i)
#define STAMP_DUMP()
struct cplx { f32x2 r, i; };
DI cplx cmul(cplx a, cplx b) { cplx c; c.r = a.r * b.r - a.i * b.i; c.i = a.r * b.i + a.i * b.r; return c; }
DI cplx csq(cplx a) { cplx c; c.r = a.r * a.r - a.i * a.i; f32x2 t = a.r * a.i; c.i = t + t; return c; }
DI cplx conj(cplx a) { cplx c; c.r = a.r; c.i = -a.i; return c; }
DI cplx cexp_p(f32x2 zr2, f32x2 zi_rev, float p) {
  cplx c;
#pragma unroll
  for (int m = 0; m < 2; m++) {
    float mag = __builtin_amdgcn_exp2f(zr2[m] * p);
    float ph = __builtin_amdgcn_fractf(zi_rev[m] * p);
    c.r[m] = mag * __builtin_amdgcn_cosf(ph);
    c.i[m] = mag * __builtin_amdgcn_sinf(ph);
  }
  return c;
}
DI void tput(lptr trow, cplx e) { u32x2 v; v[0] = cvtpk(e.r[0], e.i[0]); v[1] = cvtpk(e.r[1], e.i[1]); lds_w64(trow, v); }

typedef __amdgpu_buffer_rsrc_t rsrc_t;
DI rsrc_t mkrsrc(const void* p, u32 bytes) { return __builtin_amdgcn_make_buffer_rsrc((void*)p, 0, bytes, 0x00020000); }
DI u32 bl16(rsrc_t r, u32 off) { return __builtin_amdgcn_raw_buffer_load_b16(r, off, 0, 0); }
DI u32 bl32(rsrc_t r, u32 off) { return __builtin_amdgcn_raw_buffer_load_b32(r, off, 0, 0); }
DI u32x2 bl64(rsrc_t r, u32 off) { return __builtin_bit_cast(u32x2, __builtin_amdgcn_raw_buffer_load_b64(r, off, 0, 0)); }
DI u32x4 bl128(rsrc_t r, u32 off) { return __builtin_bit_cast(u32x4, __builtin_amdgcn_raw_buffer_load_b128(r, off, 0, 0)); }
constexpr u32 OOB = 0x80000000u;

template <int OFF> DI void rdB(u32x2& lo, u32x2& hi, u32 a) {
  asm volatile("ds_read_b64 %0, %2 offset:%3\n\tds_read_b64 %1, %2 offset:%4" : "=&v"(lo), "=&v"(hi) : "v"(a), "i"(OFF), "i"(OFF + 8) : "memory");
}
struct BF { u32x2 lo[4], hi[4]; };
template <int SG> DI void loadB(BF& b, u32 xa) {
  rdB<0 * XP_STRIDE + SG * 64>(b.lo[0], b.hi[0], xa);
  rdB<1 * XP_STRIDE + SG * 64>(b.lo[1], b.hi[1], xa);
  rdB<2 * XP_STRIDE + SG * 64>(b.lo[2], b.hi[2], xa);
  rdB<3 * XP_STRIDE + SG * 64>(b.lo[3], b.hi[3], xa);
}
DI void waitB(BF& b) {
  asm volatile("s_waitcnt lgkmcnt(0)" : "+v"(b.lo[0]), "+v"(b.hi[0]), "+v"(b.lo[1]), "+v"(b.hi[1]), "+v"(b.lo[2]), "+v"(b.hi[2]), "+v"(b.lo[3]), "+v"(b.hi[3]) :: "memory");
}
typedef f32x4 Acc[2][4][2];
template <int SG, int P0, int P1> DI void groupMM(const u32x4 (&A)[16], const BF& b, Acc& acc) {
#pragma unroll
  for (int r = 0; r < 4; r++) {
    u32x4 B = {b.lo[r][0], b.lo[r][1], b.hi[r][0], b.hi[r][1]};
#pragma unroll
    for (int sl = 0; sl < 2; sl++) {
      const int P = sl ? P1 : P0;
      if (P < 0) continue;
#pragma unroll
      for (int hh = 0; hh < 2; hh++) {
        const int c = 2 * (2 * P + hh) - (SG - 1);
        if (c >= 0) acc[sl][r][hh] = mfma(A[c], B, SG == 0 ? f32x4{0, 0, 0, 0} : acc[sl][r][hh]);
      }
    }
  }
}
template <int S, int P0, int P1, bool PRE, int NEXT> DI void block(const u32x4 (&A)[16], Acc& acc, BF& b0, u32 xa) {
  BF b1;
  if (!PRE) { loadB<4 * S>(b0, xa); waitB(b0); }
  loadB<4 * S + 1>(b1, xa);
  __builtin_amdgcn_sched_barrier(0);
  groupMM<4 * S, P0, P1>(A, b0, acc);
  __builtin_amdgcn_sched_barrier(0);
  waitB(b1);
  loadB<4 * S + 2>(b0, xa);
  __builtin_amdgcn_sched_barrier(0);
  groupMM<4 * S + 1, P0, P1>(A, b1, acc);
  __builtin_amdgcn_sched_barrier(0);
  waitB(b0);
  loadB<4 * S + 3>(b1, xa);
  __builtin_amdgcn_sched_barrier(0);
  groupMM<4 * S + 2, P0, P1>(A, b0, acc);
  __builtin_amdgcn_sched_barrier(0);
  waitB(b1);
  if (NEXT >= 0) loadB<(NEXT >= 0 ? NEXT : 0)>(b0, xa);
  __builtin_amdgcn_sched_barrier(0);
  groupMM<4 * S + 3, P0, P1>(A, b1, acc);
  __builtin_amdgcn_sched_barrier(0);
  if (NEXT >= 0) waitB(b0);
}
template <int P> DI void finish(const f32x4 (&a)[4][2], float (&carry)[3], lptr yw, int rot, bool l0) {
  float t[2][3];
#pragma unroll
  for (int hh = 0; hh < 2; hh++) {
    float c0 = a[1][hh][3] + a[2][hh][2] + a[3][hh][1], c1 = a[2][hh][3] + a[3][hh][2], c2 = a[3][hh][3];
    t[hh][0] = __int_as_float(__builtin_amdgcn_ds_bpermute(rot, __float_as_int(c0)));
    t[hh][1] = __int_as_float(__builtin_amdgcn_ds_bpermute(rot, __float_as_int(c1)));
    t[hh][2] = __int_as_float(__builtin_amdgcn_ds_bpermute(rot, __float_as_int(c2)));
  }
#pragma unroll
  for (int hh = 0; hh < 2; hh++) {
    float x[3];
#pragma unroll
    for (int k = 0; k < 3; k++) x[k] = l0 ? (hh ? t[0][k] : (P ? carry[k] : 0.f)) : t[hh][k];
    float y0 = a[0][hh][0] + x[0], y1 = a[0][hh][1] + a[1][hh][0] + x[1];
    float y2 = a[0][hh][2] + a[1][hh][1] + a[2][hh][0] + x[2], y3 = a[0][hh][3] + a[1][hh][2] + a[2][hh][1] + a[3][hh][0];
    lds_w64(yw + 512 * (2 * P + hh), u32x2{cvtpk(y0, y1), cvtpk(y2, y3)});
  }
#pragma unroll
  for (int k = 0; k < 3; k++) carry[k] = t[1][k];
}

DI void cnt_signal(lptr p, int lane) {
  asm volatile("s_waitcnt lgkmcnt(0)" ::: "memory");
  if (lane == 0) __hip_atomic_fetch_add((LDS u32*)p, 1u, __ATOMIC_RELAXED, __HIP_MEMORY_SCOPE_WORKGROUP);
  asm volatile("" ::: "memory");
}
DI void cnt_wait(lptr p, u32 target) {
  asm volatile("" ::: "memory");
#pragma unroll 1
  for (int it = 0; it < (1 << 22); it++) {
    u32 v = __builtin_amdgcn_readfirstlane(__hip_atomic_load((LDS u32*)p, __ATOMIC_RELAXED, __HIP_MEMORY_SCOPE_WORKGROUP));
    if (v >= target) break;
    __builtin_amdgcn_s_sleep(1);
  }
  asm volatile("" ::: "memory");
}

struct XIn { u32x2 k[2], v[2], h; };
DI void process_x(lptr xw, u32 sel0, u32 sel1, bool odd2, const u32x2 (&xu)[2], const XIn& in, u32x4 shw0, u32x4 shw1,
                  u32x2 (&xkv)[2]) {
  u32x2 kr[5];
#pragma unroll
  for (int d = 0; d < 2; d++) {
    u32 h1 = __builtin_amdgcn_update_dpp(0u, in.h[d], 0x101, 0xf, 0xf, true);
    u32 h2 = __builtin_amdgcn_update_dpp(0u, in.h[d], 0x102, 0xf, 0xf, true);
    kr[1][d] = __builtin_amdgcn_update_dpp(h1, in.k[0][d], 0x138, 0xf, 0xf, false);
    kr[2][d] = __builtin_amdgcn_update_dpp(h2, in.k[1][d], 0x138, 0xf, 0xf, false);
    kr[0][d] = __builtin_amdgcn_update_dpp(in.h[d], kr[2][d], 0x138, 0xf, 0xf, false);
    kr[3][d] = in.k[0][d]; kr[4][d] = in.k[1][d];
  }
#pragma unroll
  for (int cp = 0; cp < 2; cp++) {
    u32x4 sw = cp ? shw1 : shw0;
    f32x2 t0 = {blo(sw[0]), blo(sw[2])}, t1 = {bhi(sw[0]), bhi(sw[2])}, t2 = {blo(sw[1]), blo(sw[3])}, t3 = {bhi(sw[1]), bhi(sw[3])};
    f32x2 kf[5];
#pragma unroll
    for (int e = 0; e < 5; e++) kf[e] = f32x2{blo(kr[e][cp]), bhi(kr[e][cp])};
#pragma unroll
    for (int e = 0; e < 2; e++) {
      f32x2 p3 = kf[e] * t3, p2 = kf[e + 1] * t2, p1 = kf[e + 2] * t1, p0 = kf[e + 3] * t0;
      u32 u3 = cvtpk(p3[0], p3[1]), u2 = cvtpk(p2[0], p2[1]), u1 = cvtpk(p1[0], p1[1]), u0 = cvtpk(p0[0], p0[1]);
      f32x2 s = f32x2{blo(u3), bhi(u3)} + f32x2{blo(u2), bhi(u2)};
      u32 us = cvtpk(s[0], s[1]);
      s = f32x2{blo(us), bhi(us)} + f32x2{blo(u1), bhi(u1)}; us = cvtpk(s[0], s[1]);
      s = f32x2{blo(us), bhi(us)} + f32x2{blo(u0), bhi(u0)}; us = cvtpk(s[0], s[1]);
      s = f32x2{blo(us), bhi(us)} * f32x2{blo(in.v[e][cp]), bhi(in.v[e][cp])};
      xkv[e][cp] = cvtpk(s[0], s[1]);
    }
  }
#pragma unroll
  for (int arr = 0; arr < 2; arr++) {
#pragma unroll
    for (int a = 0; a < 2; a++) {
      u32x2 x = arr ? xkv[a] : xu[a];
      u32 keep = odd2 ? x[1] : x[0], send = odd2 ? x[0] : x[1];
      u32 other = __builtin_amdgcn_update_dpp(0u, send, 0x4e, 0xf, 0xf, true);
      lds_w32(xw + arr * 4 * XB_STRIDE + a * XP_STRIDE, perm(other, keep, sel0));
      lds_w32(xw + (arr * 4 + 1) * XB_STRIDE + a * XP_STRIDE, perm(other, keep, sel1));
    }
  }
}

DI void epilogue(lptr yr, u32 ooff, u32 tag, const u64* gp, u64 gr, const u32x2 (&xu)[2], const u32x2 (&xkv)[2], u32x2 dsk,
                 u32x2 dss, rsrc_t rout) {
  u32 ya[4], yb[4];
#pragma unroll
  for (int c = 0; c < 4; c++) {
    ya[c] = *(LDS u32*)(yr + c * KB_STRIDE);
    yb[c] = *(LDS u32*)(yr + (4 + c) * KB_STRIDE);
  }
  if (!__all((u32)(gr >> 32) == tag)) {
#pragma unroll 1
    for (int it = 0; it < (1 << 16); it++) {
      __builtin_amdgcn_s_sleep(2);
      gr = __hip_atomic_load(gp, __ATOMIC_RELAXED, __HIP_MEMORY_SCOPE_AGENT);
      if (__all((u32)(gr >> 32) == tag)) break;
    }
  }
#pragma unroll
  for (int e = 0; e < 2; e++) {
    float g = e ? bhi((u32)gr) : blo((u32)gr), g1 = rb(1.f - g);
    u32x2 o;
#pragma unroll
    for (int cp = 0; cp < 2; cp++) {
      float res[2];
#pragma unroll
      for (int hl = 0; hl < 2; hl++) {
        int c = cp * 2 + hl;
        float uu = hl ? bhi(xu[e][cp]) : blo(xu[e][cp]);
        float kv = hl ? bhi(xkv[e][cp]) : blo(xkv[e][cp]);
        float d1 = hl ? bhi(dsk[cp]) : blo(dsk[cp]);
        float d2 = hl ? bhi(dss[cp]) : blo(dss[cp]);
        float y1 = e ? bhi(ya[c]) : blo(ya[c]);
        float y2 = e ? bhi(yb[c]) : blo(yb[c]);
        float a = rb(y1 + rb(uu * d1));
        float bb = rb(uu * rb(y2 + rb(kv * d2)));
        res[hl] = rb(g * a) + rb(g1 * bb);
      }
      o[cp] = cvtpk(res[0], res[1]);
    }
    __builtin_amdgcn_raw_buffer_store_b64(__builtin_bit_cast(__attribute__((ext_vector_type(2))) unsigned, o), rout, ooff + e * 2048, 0, 0);
  }
}

template <class F> DI void conv(const u32x4 (&A)[16], u32 xa, lptr yw, int rot, int lane, lptr cnt, F hook) {
  Acc acc; BF b0; float carry[3];
  const bool l0 = lane == 0;
  cnt_wait(cnt + 0, 4);
  block<0, 0, 1, false, 0>(A, acc, b0, xa);
  finish<0>(acc[0], carry, yw, rot, l0);
  cnt_signal(cnt + 16, lane);
  hook(std::integral_constant<int, 0>{});
  block<0, 2, -1, true, -1>(A, acc, b0, xa);
  hook(std::integral_constant<int, 1>{});
  cnt_wait(cnt + 4, 4);
  block<1, 2, 1, false, 0>(A, acc, b0, xa);
  finish<1>(acc[1], carry, yw, rot, l0);
  cnt_signal(cnt + 20, lane);
  hook(std::integral_constant<int, 2>{});
  block<0, -1, 3, true, 4>(A, acc, b0, xa);
  hook(std::integral_constant<int, 3>{});
  block<1, -1, 3, true, -1>(A, acc, b0, xa);
  hook(std::integral_constant<int, 4>{});
  cnt_wait(cnt + 8, 4);
  block<2, 2, 3, false, -1>(A, acc, b0, xa);
  finish<2>(acc[0], carry, yw, rot, l0);
  cnt_signal(cnt + 24, lane);
  hook(std::integral_constant<int, 5>{});
  cnt_wait(cnt + 12, 4);
  block<3, -1, 3, false, -1>(A, acc, b0, xa);
  finish<3>(acc[1], carry, yw, rot, l0);
  cnt_signal(cnt + 28, lane);
}

__global__ __launch_bounds__(512, 2) void conv_kernel(
    const void* __restrict__ gu, const void* __restrict__ logAr, const void* __restrict__ Aim, const void* __restrict__ Cre,
    const void* __restrict__ Cim, const void* __restrict__ logdt, const void* __restrict__ Dskip, const void* __restrict__ gk,
    const void* __restrict__ gv, const void* __restrict__ shk, const void* __restrict__ ssmk, const void* __restrict__ ssmD,
    const void* __restrict__ gatew, void* ws, void* __restrict__ out) {
  extern __shared__ __attribute__((aligned(16))) char smem_[];
  lptr smem = (lptr)smem_;
  const int b = blockIdx.x, cg = (b & 7) * 32 + (b >> 3);
  const int tid = threadIdx.x, lane = tid & 63, w = __builtin_amdgcn_readfirstlane(tid >> 6);
  const int ch = cg * 4 + (w & 3);
  const int j16 = lane & 15, q = lane >> 4;
  const bool isA = w < 4;
  lptr kb = smem + OFF_K + w * KB_STRIDE;
  u64* gq = (u64*)ws;
  u32* gepoch = (u32*)((char*)ws + 65536);
  const rsrc_t ru = mkrsrc(gu, L * D * 2), rk = mkrsrc(gk, L * D * 2), rv = mkrsrc(gv, L * D * 2), rout = mkrsrc(out, L * D * 2);
  STAMP(0);
  lptr cnt = smem + OFF_MISC;
  u64 pc;
  asm volatile("s_getpc_b64 %0" : "=s"(pc));
  if (tid < 16) lds_w32(cnt + tid * 4, 0);
  asm volatile("s_waitcnt lgkmcnt(0)\n\ts_barrier" ::: "memory");
  auto loadA = [&](u32x4 (&A)[16]) {
    const int m = lane & 3, ep = (lane >> 2) & 3;
    lptr ka = kb + 32 + (-12 + 4 * m + 4 * (8 * q + 7 - ep)) * 2;
#pragma unroll
    for (int c = 0; c < 16; c++) {
      u32x2 lo = lds_tr(ka + c * 256), hi = lds_tr(ka + c * 256 - 32);
      A[c] = u32x4{lo[0], lo[1], hi[0], hi[1]};
    }
  };
  const u32 xa = (u32)(size_t)(smem + OFF_X + w * XB_STRIDE + (64 - 32 + 4 * j16 - 8 * q - 4) * 2);
  const int rot = (lane == 0 ? 63 : q == 0 ? lane + 47 : lane - 16) * 4;
  lptr yw = kb + 32 + (16 * j16 + 4 * q) * 2;
  u32x4 A[16];
  const int tb = tid & 255, wb = w & 3;
  const u32 xoff = (2 * tb * 256 + cg) * 8;
  u32x2 xu[4][2], xkv[4][2];
  XIn in[4];
  auto load_x = [&](int s) {
    const u32 o = xoff + s * 512 * 2048;
    const int hrow = 512 * s + 128 * wb - 3 + lane;
    in[s].k[0] = bl64(rk, o); in[s].k[1] = bl64(rk, o + 2048);
    in[s].h = bl64(rk, (lane < 3 && hrow >= 0) ? (u32)(hrow * 256 + cg) * 8 : OOB);
    in[s].v[0] = bl64(rv, o); in[s].v[1] = bl64(rv, o + 2048);
    xu[s][0] = bl64(ru, o); xu[s][1] = bl64(ru, o + 2048);
    __builtin_amdgcn_sched_barrier(0);
  };
  auto landed = [&](int s) { asm volatile("" : "+v"(xu[s][1])); __builtin_amdgcn_sched_barrier(0); };
  const bool odd2 = lane & 2;
  const u32 sel0 = odd2 ? 0x01000504u : 0x05040100u, sel1 = odd2 ? 0x03020706u : 0x07060302u;
  lptr xw = smem + OFF_X + (odd2 ? 2 * XB_STRIDE : 0) + (lane & 1) * 2 * XP_STRIDE + 128 + (tb >> 2) * 4;
  u32x4 shw0, shw1; u32x2 dsk, dss;
  auto load_par = [&]() {
    const u32* p1 = (const u32*)shk + cg * 8;
#pragma unroll
    for (int e = 0; e < 4; e++) { shw0[e] = p1[e]; shw1[e] = p1[4 + e]; }
#pragma unroll
    for (int e = 0; e < 2; e++) { dsk[e] = ((const u32*)Dskip)[cg * 2 + e]; dss[e] = ((const u32*)ssmD)[cg * 2 + e]; }
  };
  auto proc = [&](int s) {
    process_x(xw + s * 256, sel0, sel1, odd2, xu[s], in[s], shw0, shw1, xkv[s]);
    cnt_signal(cnt + 4 * s, lane);
  };
  lptr yr = smem + OFF_K + 32 + tb * 4;
  const u64* gp = gq + tb;
  u64 gr[4];
  auto ldg = [&](int s) { gr[s] = __hip_atomic_load(gp + s * 256, __ATOMIC_RELAXED, __HIP_MEMORY_SCOPE_AGENT); };
  auto epi = [&](int s, u32 tag) {
    cnt_wait(cnt + 16 + 4 * s, 8);
    epilogue(yr + s * 1024, xoff + s * 512 * 2048, tag, gp + s * 256, gr[s], xu[s], xkv[s], dsk, dss, rout);
  };
  auto retire = [&](u32 tag) {
    if (b == 0 && lane == 0) {
      u32 n = __hip_atomic_fetch_add((LDS u32*)(cnt + 32), 1u, __ATOMIC_RELAXED, __HIP_MEMORY_SCOPE_WORKGROUP);
      if (n == 7) __hip_atomic_store(gepoch, tag, __ATOMIC_RELAXED, __HIP_MEMORY_SCOPE_AGENT);
    }
  };

  if (isA) {
    const rsrc_t r1 = mkrsrc(logAr, D * NM * 2), r2 = mkrsrc(Aim, D * NM * 2), r3 = mkrsrc(Cre, D * NM * 2), r4 = mkrsrc(Cim, D * NM * 2);
    const u32 po = (ch * 64 + lane) * 4;
    u32 pAr = bl32(r1, po), pAi = bl32(r2, po), pCr = bl32(r3, po), pCi = bl32(r4, po);
    const u32 pdt = (((const u32*)logdt)[ch >> 1] >> ((ch & 1) * 16)) & 0xffffu;
    load_par();
    const rsrc_t rw = mkrsrc(gatew, D * 2);
    const int grow = b * 8 + wb * 2;
    u32x4 gx[4], gw[2];
#pragma unroll
    for (int e = 0; e < 4; e++) gx[e] = bl128(ru, grow * 2048 + (e * 64 + lane) * 16);
#pragma unroll
    for (int e = 0; e < 2; e++) gw[e] = bl128(rw, (e * 64 + lane) * 16);
    const u32 tag = __hip_atomic_load(gepoch, __ATOMIC_RELAXED, __HIP_MEMORY_SCOPE_AGENT) + 1;
    __builtin_amdgcn_sched_barrier(0);
    auto gate = [&]() {
      u32 gb[2];
#pragma unroll
      for (int rr = 0; rr < 2; rr++) {
        float s = 0.f;
#pragma unroll
        for (int h = 0; h < 2; h++) {
          u32x4 x = gx[rr * 2 + h], ww = gw[h];
#pragma unroll
          for (int e = 0; e < 4; e++) { u32 p = cvtpk(blo(x[e]) * blo(ww[e]), bhi(x[e]) * bhi(ww[e])); s += blo(p) + bhi(p); }
        }
#pragma unroll
        for (int o = 32; o >= 1; o >>= 1) s += __shfl_xor(s, o);
        float z = rb(s), g = 1.f / (1.f + expf(-z));
        gb[rr] = cvtpk(g, g) & 0xffffu;
      }
      if (lane == 0) __hip_atomic_store(gq + (grow >> 1), ((u64)tag << 32) | (gb[0] | gb[1] << 16), __ATOMIC_RELAXED, __HIP_MEMORY_SCOPE_AGENT);
    };
    if (w == 0) {
      __builtin_amdgcn_raw_ptr_buffer_load_lds(mkrsrc((const void*)(pc & ~127ull), CODE_BYTES), (LDS void*)(smem + OFF_PF + 4096), 4,
                                               lane < 8 ? ((b >> 3) * 8 + lane) * 128 : OOB, 0, 0, 0);
    }
    STAMP(10);
    if (PRIO & 1) __builtin_amdgcn_s_setprio(1);
    float dt = rb(expf(bf(pdt)));
    f32x2 zr, zi; cplx C;
    zr[0] = -rb(expf(blo(pAr))) * dt; zr[1] = -rb(expf(bhi(pAr))) * dt;
    zi[0] = blo(pAi) * dt; zi[1] = bhi(pAi) * dt;
    C.r[0] = 2.f * blo(pCr) * dt; C.r[1] = 2.f * bhi(pCr) * dt;
    C.i[0] = -2.f * blo(pCi) * dt; C.i[1] = -2.f * bhi(pCi) * dt;
    zr *= 1.44269504088896341f; zi *= 0.15915494309189535f;
    STAMP(11);
    lptr tb = smem + OFF_T + w * TB_STRIDE;
    lptr tw = tb + lane * 8, tr = tb + j16 * TB_ROW + q * 16;
    cplx w1 = cexp_p(zr, zi, 1.f), w16 = cexp_p(zr, zi, 16.f), w256 = cexp_p(zr, zi, 256.f);
    u32x4 G[8];
    {
      cplx g; g.r = f32x2{1.f, 1.f}; g.i = f32x2{0.f, 0.f};
      tput(tw, g); g = w16; tput(tw + TB_ROW, g);
#pragma unroll
      for (int i = 2; i < 16; i++) { g = cmul(g, w16); tput(tw + i * TB_ROW, g); }
      wsync();
#pragma unroll
      for (int s = 0; s < 8; s++) G[s] = lds_r128(tr + s * 64);
      wsync();
    }
    STAMP(12);
    if (w < 3) {
      const rsrc_t rp = w == 0 ? rk : w == 1 ? rv : ru;
      const u32 pf = (512 + (b >> 3) * 48 + (lane >> 2)) * 2048 + (b & 7) * 256 + (lane & 3) * 64;
      LDS void* sink = (LDS void*)(smem + OFF_PF + w * 256);
#pragma unroll
      for (int i = 0; i < 3; i++) __builtin_amdgcn_raw_ptr_buffer_load_lds(rp, sink, 4, pf + i * 16 * 2048, 0, 0, 0);
    }
    w1 = conj(w1); w256 = conj(w256);
    cplx w2 = csq(w1), w4 = csq(w2), w8 = csq(w4), w512 = csq(w256), w1024 = csq(w512);
    lptr kd = kb + 32;
    if (lane < 8) lds_w32(kb + lane * 4, 0);
    {
      cplx e = C; tput(tw, e);
#pragma unroll
      for (int i = 1; i < 16; i++) { e = cmul(e, w1); tput(tw + i * TB_ROW, e); }
      f32x4 a = {0, 0, 0, 0};
      wsync();
#pragma unroll
      for (int s = 0; s < 8; s++) a = mfma(lds_r128(tr + s * 64), G[s], a);
      wsync();
      lds_w64(kd + (16 * j16 + 4 * q) * 2, u32x2{cvtpk(a[0], a[1]), cvtpk(a[2], a[3])});
    }
    STAMP(13);
    {
      cplx e = cmul(C, w256); tput(tw, e);
#pragma unroll
      for (int i = 1; i < 8; i++) { e = cmul(e, w2); tput(tw + i * TB_ROW, e); }
      e = cmul(C, w512);
#pragma unroll
      for (int i = 0; i < 4; i++) { tput(tw + (8 + i) * TB_ROW, e); tput(tw + (12 + i) * TB_ROW, cmul(e, w256)); if (i < 3) e = cmul(e, w4); }
      f32x4 a = {0, 0, 0, 0};
      wsync();
#pragma unroll
      for (int s = 0; s < 8; s++) a = mfma(lds_r128(tr + s * 64), G[s], a);
      wsync();
      u32 p01 = cvtpk(a[0], a[1]), p23 = cvtpk(a[2], a[3]);
      u32 v00 = perm(p01, p01, 0x01000100), v11 = perm(p01, p01, 0x03020302), v22 = perm(p23, p23, 0x01000100), v33 = perm(p23, p23, 0x03020302);
      const int base = q < 2 ? 256 + 16 * j16 + 8 * q : 512 + 256 * (q - 2) + 16 * j16;
      lds_w128(kd + base * 2, q < 2 ? u32x4{v00, perm(p23, p01, 0x05040302), v22, v33} : u32x4{v00, p01, v11, v22});
      if (q >= 2) lds_w128(kd + base * 2 + 16, u32x4{v22, p23, v33, v33});
      wsync();
      if (q >= 2 && base > 512) lds_w32(kd + (base - 2) * 2, v00); else if (base > 256) lds_w16(kd + (base - 1) * 2, (u16)p01);
      wsync();
    }
    STAMP(14);
    {
      cplx e0 = cmul(C, w1024), e1 = cmul(e0, w8);
#pragma unroll
      for (int i = 0; i < 4; i++) { tput(tw + (2 * i) * TB_ROW, e0); tput(tw + (2 * i + 1) * TB_ROW, e1); e0 = cmul(e0, w256); if (i < 3) e1 = cmul(e1, w256); }
      tput(tw + 8 * TB_ROW, e0);
      f32x4 a = {0, 0, 0, 0};
      wsync();
#pragma unroll
      for (int s = 0; s < 8; s++) a = mfma(lds_r128(tr + s * 64), G[s], a);
      wsync();
      u32 p01 = cvtpk(a[0], a[1]), p23 = cvtpk(a[2], a[3]);
      if (q < 2) {
#pragma unroll
        for (int hh = 0; hh < 2; hh++) {
          u32 p = hh ? p23 : p01;
          u32 v00 = perm(p, p, 0x01000100), v11 = perm(p, p, 0x03020302);
          int base = 1024 + 512 * q + 256 * hh + 16 * j16;
          lds_w128(kd + base * 2, u32x4{v00, v00, p, v11});
          lds_w128(kd + base * 2 + 16, u32x4{v11, v11, v11, v11});
        }
      }
      wsync();
      if (q < 2) {
#pragma unroll
        for (int hh = 0; hh < 2; hh++) {
          u32 p = hh ? p23 : p01;
          u32 v00 = perm(p, p, 0x01000100);
          int base = 1024 + 512 * q + 256 * hh + 16 * j16;
          if (base > 1024) lds_w64(kd + (base - 4) * 2, u32x2{v00, v00}); else lds_w32(kd + (base - 2) * 2, v00);
        }
      } else if (lane == 32) {
        u32 v00 = perm(p01, p01, 0x01000100);
        lds_w64(kd + 2044 * 2, u32x2{v00, v00});
      }
      wsync();
    }
    STAMP(15);
    if (PRIO & 1) __builtin_amdgcn_s_setprio(0);
    if (lane == 0) lds_w32(cnt + 36, tag);
    loadA(A);
    STAMP(1);
    conv(A, xa, yw, rot, lane, cnt, [&](auto k) {
      constexpr int K = k.value;
      STAMP(2 + K);
      if (PRIO & 8) __builtin_amdgcn_s_setprio(1);
      if (K == GATE_AT) gate();
      if (K == X3_AT) load_x(3);
      if (K == P3_AT) proc(3);
      if (K == 4) ldg(3);
      if (PRIO & 8) __builtin_amdgcn_s_setprio(0);
    });
    STAMP(8);
    epi(3, tag);
    retire(tag);
    STAMP(9);
    STAMP_DUMP();
  } else {
    const rsrc_t rs = mkrsrc(ssmk, L * D * 2);
    load_x(0);
    u32x4 kk[4];
#pragma unroll
    for (int e = 0; e < 4; e++) kk[e] = bl128(rs, ch * 4096 + (e * 64 + lane) * 16);
    load_par();
    __builtin_amdgcn_sched_barrier(0);
    STAMP(10);
    for (int i = tb; i < 32 * 32; i += 256) lds_w32(smem + OFF_X + (i >> 7) * XB_STRIDE + ((i >> 5) & 3) * XP_STRIDE + (i & 31) * 4, 0);
    if (lane < 8) lds_w32(kb + lane * 4, 0);
    STAMP(11);
    landed(0);
    STAMP(12);
    load_x(1);
    proc(0);
    STAMP(13);
#pragma unroll
    for (int e = 0; e < 4; e++) lds_w128(kb + 32 + (e * 64 + lane) * 16, kk[e]);
    wsync();
    STAMP(14);
    STAMP(15);
    loadA(A);
    STAMP(1);
    u32 tag = 0;
    if (PRIO & 2) __builtin_amdgcn_s_setprio(1);
    conv(A, xa, yw, rot, lane, cnt, [&](auto k) {
      constexpr int K = k.value;
      STAMP(2 + K);
      if (PRIO & 4) __builtin_amdgcn_s_setprio(1);
      if (K == 0) { landed(1); load_x(2); proc(1); }
      if (K == 2) proc(2);
      if (K == 3) { ldg(0); ldg(1); ldg(2); }
      if (K == 4) {
        cnt_wait(cnt + 16, 8);
        tag = __builtin_amdgcn_readfirstlane(*(LDS u32*)(cnt + 36));
        epi(0, tag);
      }
      if (K == 5) epi(1, tag);
      if (PRIO & 4) __builtin_amdgcn_s_setprio(0);
    });
    STAMP(8);
    epi(2, tag);
    retire(tag);
    STAMP(9);
    STAMP_DUMP();
  }
}

extern "C" void kernel_launch(void* const* d_in, const int* in_sizes, int n_in, void* d_out, int out_size, void* d_ws,
                              size_t ws_size, hipStream_t stream) {
  (void)hipFuncSetAttribute((const void*)conv_kernel, hipFuncAttributeMaxDynamicSharedMemorySize, LDS_BYTES);
  conv_kernel<<<256, 512, LDS_BYTES, stream>>>(d_in[0], d_in[1], d_in[2], d_in[3], d_in[4], d_in[5], d_in[6], d_in[8], d_in[9],
                                               d_in[10], d_in[11], d_in[12], d_in[13], d_ws, d_out);
}
```

```cpp
#include <hip/hip_runtime.h>
#include <hip/hip_bf16.h>
#include <type_traits>

typedef unsigned int u32;
typedef unsigned short u16;
typedef unsigned long long u64;
typedef u32 u32x2 __attribute__((ext_vector_type(2)));
typedef u32 u32x4 __attribute__((ext_vector_type(4)));
typedef float f32x2 __attribute__((ext_vector_type(2)));
typedef float f32x4 __attribute__((ext_vector_type(4)));
typedef __bf16 bf16x2 __attribute__((ext_vector_type(2)));
typedef __bf16 bf16x8 __attribute__((ext_vector_type(8)));
typedef short s16x4 __attribute__((ext_vector_type(4)));
#define DI __device__ __forceinline__
#define LDS __attribute__((address_space(3)))

constexpr int L = 2048, D = 1024, NM = 128;
constexpr int KB_STRIDE = 4128;
constexpr int XP_STRIDE = 1184;
constexpr int XB_STRIDE = 4 * XP_STRIDE + 64;
constexpr int TB_ROW = 528;
constexpr int TB_STRIDE = 16 * TB_ROW;
constexpr int OFF_K = 0;
constexpr int OFF_X = OFF_K + 8 * KB_STRIDE;
constexpr int OFF_T = OFF_X + 8 * XB_STRIDE;
constexpr int OFF_MISC = OFF_T + 4 * TB_STRIDE;
constexpr int OFF_PF = OFF_MISC + 64;
constexpr int LDS_BYTES = OFF_PF + 4096 + 256;
constexpr int CODE_BYTES = 37376;
#define PRIO 18
#define X3_AT 1
#define P3_AT 3
#define PDROP 5
#define GATE_AT 0
#define CODEPF 1
#define PREFETCH 0

DI u32 cvtpk(float lo, float hi) { f32x2 v = {lo, hi}; bf16x2 b = __builtin_convertvector(v, bf16x2); return __builtin_bit_cast(u32, b); }
DI float blo(u32 x) { return __uint_as_float(x << 16); }
DI float bhi(u32 x) { return __uint_as_float(x & 0xffff0000u); }
DI float rb(float x) { return blo(cvtpk(x, x)); }
DI float bf(u16 x) { return __uint_as_float((u32)x << 16); }
DI f32x4 mfma(u32x4 a, u32x4 b, f32x4 c) {
  return __builtin_amdgcn_mfma_f32_16x16x32_bf16(__builtin_bit_cast(bf16x8, a), __builtin_bit_cast(bf16x8, b), c, 0, 0, 0);
}
typedef LDS char* lptr;
DI u32x2 lds_r64(lptr p) { return *(LDS u32x2*)p; }
DI u32x4 lds_r128(lptr p) { return *(LDS u32x4*)p; }
DI void lds_w32(lptr p, u32 v) { *(LDS u32*)p = v; }
DI void lds_w16(lptr p, u16 v) { *(LDS u16*)p = v; }
DI void lds_w64(lptr p, u32x2 v) { *(LDS u32x2*)p = v; }
DI void lds_w128(lptr p, u32x4 v) { *(LDS u32x4*)p = v; }
DI u32x2 lds_tr(lptr p) {
  typedef short v4i16_t __attribute__((ext_vector_type(4)));
  return __builtin_bit_cast(u32x2, __builtin_amdgcn_ds_read_tr16_b64_v4i16((LDS v4i16_t*)p));
}
DI void wsync() { __builtin_amdgcn_fence(__ATOMIC_ACQ_REL, "wavefront"); __builtin_amdgcn_wave_barrier(); }
DI u32 perm(u32 hi, u32 lo, u32 sel) { return __builtin_amdgcn_perm(hi, lo, sel); }

#define STAMP(i)
#define STAMP_DUMP()
template <int CTRL> DI float dpp_add(float s) { return s + __int_as_float(__builtin_amdgcn_update_dpp(0, __float_as_int(s), CTRL, 0xf, 0xf, true)); }
DI float wave_sum(float s) {
  s = dpp_add<0xb1>(s); s = dpp_add<0x4e>(s); s = dpp_add<0x141>(s); s = dpp_add<0x140>(s);
  const int si = __float_as_int(s);
  return (__int_as_float(__builtin_amdgcn_readlane(si, 0)) + __int_as_float(__builtin_amdgcn_readlane(si, 16))) +
         (__int_as_float(__builtin_amdgcn_readlane(si, 32)) + __int_as_float(__builtin_amdgcn_readlane(si, 48)));
}
struct cplx { f32x2 r, i; };
DI cplx cmul(cplx a, cplx b) { cplx c; c.r = a.r * b.r - a.i * b.i; c.i = a.r * b.i + a.i * b.r; return c; }
DI cplx csq(cplx a) { cplx c; c.r = a.r * a.r - a.i * a.i; f32x2 t = a.r * a.i; c.i = t + t; return c; }
DI cplx conj(cplx a) { cplx c; c.r = a.r; c.i = -a.i; return c; }
DI cplx cexp_p(f32x2 zr2, f32x2 zi_rev, float p) {
  cplx c;
#pragma unroll
  for (int m = 0; m < 2; m++) {
    float mag = __builtin_amdgcn_exp2f(zr2[m] * p);
    float ph = __builtin_amdgcn_fractf(zi_rev[m] * p);
    c.r[m] = mag * __builtin_amdgcn_cosf(ph);
    c.i[m] = mag * __builtin_amdgcn_sinf(ph);
  }
  return c;
}
DI void tput(lptr trow, cplx e) { u32x2 v; v[0] = cvtpk(e.r[0], e.i[0]); v[1] = cvtpk(e.r[1], e.i[1]); lds_w64(trow, v); }

typedef __amdgpu_buffer_rsrc_t rsrc_t;
DI rsrc_t mkrsrc(const void* p, u32 bytes) { return __builtin_amdgcn_make_buffer_rsrc((void*)p, 0, bytes, 0x00020000); }
DI u32 bl16(rsrc_t r, u32 off) { return __builtin_amdgcn_raw_buffer_load_b16(r, off, 0, 0); }
DI u32 bl32(rsrc_t r, u32 off) { return __builtin_amdgcn_raw_buffer_load_b32(r, off, 0, 0); }
DI u32x2 bl64(rsrc_t r, u32 off) { return __builtin_bit_cast(u32x2, __builtin_amdgcn_raw_buffer_load_b64(r, off, 0, 0)); }
DI u32x4 bl128(rsrc_t r, u32 off) { return __builtin_bit_cast(u32x4, __builtin_amdgcn_raw_buffer_load_b128(r, off, 0, 0)); }
constexpr u32 OOB = 0x80000000u;

template <int OFF> DI void rdB(u32x2& lo, u32x2& hi, u32 a) {
  asm volatile("ds_read_b64 %0, %2 offset:%3\n\tds_read_b64 %1, %2 offset:%4" : "=&v"(lo), "=&v"(hi) : "v"(a), "i"(OFF), "i"(OFF + 8) : "memory");
}
struct BF { u32x2 lo[4], hi[4]; };
template <int SG> DI void loadB(BF& b, u32 xa) {
  rdB<0 * XP_STRIDE + SG * 64>(b.lo[0], b.hi[0], xa);
  rdB<1 * XP_STRIDE + SG * 64>(b.lo[1], b.hi[1], xa);
  rdB<2 * XP_STRIDE + SG * 64>(b.lo[2], b.hi[2], xa);
  rdB<3 * XP_STRIDE + SG * 64>(b.lo[3], b.hi[3], xa);
}
DI void waitB(BF& b) {
  asm volatile("s_waitcnt lgkmcnt(0)" : "+v"(b.lo[0]), "+v"(b.hi[0]), "+v"(b.lo[1]), "+v"(b.hi[1]), "+v"(b.lo[2]), "+v"(b.hi[2]), "+v"(b.lo[3]), "+v"(b.hi[3]) :: "memory");
}
typedef f32x4 Acc[2][4][2];
template <int SG, int P0, int P1> DI void groupMM(const u32x4 (&A)[16], const BF& b, Acc& acc) {
#pragma unroll
  for (int r = 0; r < 4; r++) {
    u32x4 B = {b.lo[r][0], b.lo[r][1], b.hi[r][0], b.hi[r][1]};
#pragma unroll
    for (int sl = 0; sl < 2; sl++) {
      const int P = sl ? P1 : P0;
      if (P < 0) continue;
#pragma unroll
      for (int hh = 0; hh < 2; hh++) {
        const int c = 2 * (2 * P + hh) - (SG - 1);
        if (c >= 0) acc[sl][r][hh] = mfma(A[c], B, SG == 0 ? f32x4{0, 0, 0, 0} : acc[sl][r][hh]);
      }
    }
  }
}
template <int S, int P0, int P1, bool PRE, int NEXT> DI void block(const u32x4 (&A)[16], Acc& acc, BF& b0, u32 xa) {
  BF b1;
  if (!PRE) { loadB<4 * S>(b0, xa); waitB(b0); }
  loadB<4 * S + 1>(b1, xa);
  __builtin_amdgcn_sched_barrier(0);
  groupMM<4 * S, P0, P1>(A, b0, acc);
  __builtin_amdgcn_sched_barrier(0);
  waitB(b1);
  loadB<4 * S + 2>(b0, xa);
  __builtin_amdgcn_sched_barrier(0);
  groupMM<4 * S + 1, P0, P1>(A, b1, acc);
  __builtin_amdgcn_sched_barrier(0);
  waitB(b0);
  loadB<4 * S + 3>(b1, xa);
  __builtin_amdgcn_sched_barrier(0);
  groupMM<4 * S + 2, P0, P1>(A, b0, acc);
  __builtin_amdgcn_sched_barrier(0);
  waitB(b1);
  if (NEXT >= 0) loadB<(NEXT >= 0 ? NEXT : 0)>(b0, xa);
  __builtin_amdgcn_sched_barrier(0);
  groupMM<4 * S + 3, P0, P1>(A, b1, acc);
  __builtin_amdgcn_sched_barrier(0);
  if (NEXT >= 0) waitB(b0);
}
template <int P> DI void finish(const f32x4 (&a)[4][2], float (&carry)[3], lptr yw, int rot, bool l0) {
  float t[2][3];
#pragma unroll
  for (int hh = 0; hh < 2; hh++) {
    float c0 = a[1][hh][3] + a[2][hh][2] + a[3][hh][1], c1 = a[2][hh][3] + a[3][hh][2], c2 = a[3][hh][3];
    t[hh][0] = __int_as_float(__builtin_amdgcn_ds_bpermute(rot, __float_as_int(c0)));
    t[hh][1] = __int_as_float(__builtin_amdgcn_ds_bpermute(rot, __float_as_int(c1)));
    t[hh][2] = __int_as_float(__builtin_amdgcn_ds_bpermute(rot, __float_as_int(c2)));
  }
#pragma unroll
  for (int hh = 0; hh < 2; hh++) {
    float x[3];
#pragma unroll
    for (int k = 0; k < 3; k++) x[k] = l0 ? (hh ? t[0][k] : (P ? carry[k] : 0.f)) : t[hh][k];
    float y0 = a[0][hh][0] + x[0], y1 = a[0][hh][1] + a[1][hh][0] + x[1];
    float y2 = a[0][hh][2] + a[1][hh][1] + a[2][hh][0] + x[2], y3 = a[0][hh][3] + a[1][hh][2] + a[2][hh][1] + a[3][hh][0];
    lds_w64(yw + 512 * (2 * P + hh), u32x2{cvtpk(y0, y1), cvtpk(y2, y3)});
  }
#pragma unroll
  for (int k = 0; k < 3; k++) carry[k] = t[1][k];
}

DI void cnt_signal(lptr p, int lane) {
  asm volatile("s_waitcnt lgkmcnt(0)" ::: "memory");
  if (lane == 0) __hip_atomic_fetch_add((LDS u32*)p, 1u, __ATOMIC_RELAXED, __HIP_MEMORY_SCOPE_WORKGROUP);
  asm volatile("" ::: "memory");
}
DI void cnt_wait(lptr p, u32 target) {
  asm volatile("" ::: "memory");
#pragma unroll 1
  for (int it = 0; it < (1 << 22); it++) {
    u32 v = __builtin_amdgcn_readfirstlane(__hip_atomic_load((LDS u32*)p, __ATOMIC_RELAXED, __HIP_MEMORY_SCOPE_WORKGROUP));
    if (v >= target) break;
    __builtin_amdgcn_s_sleep(1);
  }
  asm volatile("" ::: "memory");
}

struct XIn { u32x2 k[2], v[2], h; };
DI void process_x(lptr xw, u32 sel0, u32 sel1, bool odd2, const u32x2 (&xu)[2], const XIn& in, u32x4 shw0, u32x4 shw1,
                  u32x2 (&xkv)[2]) {
  u32x2 kr[5];
#pragma unroll
  for (int d = 0; d < 2; d++) {
    u32 h1 = __builtin_amdgcn_update_dpp(0u, in.h[d], 0x101, 0xf, 0xf, true);
    u32 h2 = __builtin_amdgcn_update_dpp(0u, in.h[d], 0x102, 0xf, 0xf, true);
    kr[1][d] = __builtin_amdgcn_update_dpp(h1, in.k[0][d], 0x138, 0xf, 0xf, false);
    kr[2][d] = __builtin_amdgcn_update_dpp(h2, in.k[1][d], 0x138, 0xf, 0xf, false);
    kr[0][d] = __builtin_amdgcn_update_dpp(in.h[d], kr[2][d], 0x138, 0xf, 0xf, false);
    kr[3][d] = in.k[0][d]; kr[4][d] = in.k[1][d];
  }
#pragma unroll
  for (int cp = 0; cp < 2; cp++) {
    u32x4 sw = cp ? shw1 : shw0;
    f32x2 t0 = {blo(sw[0]), blo(sw[2])}, t1 = {bhi(sw[0]), bhi(sw[2])}, t2 = {blo(sw[1]), blo(sw[3])}, t3 = {bhi(sw[1]), bhi(sw[3])};
    f32x2 kf[5];
#pragma unroll
    for (int e = 0; e < 5; e++) kf[e] = f32x2{blo(kr[e][cp]), bhi(kr[e][cp])};
#pragma unroll
    for (int e = 0; e < 2; e++) {
      f32x2 p3 = kf[e] * t3, p2 = kf[e + 1] * t2, p1 = kf[e + 2] * t1, p0 = kf[e + 3] * t0;
      u32 u3 = cvtpk(p3[0], p3[1]), u2 = cvtpk(p2[0], p2[1]), u1 = cvtpk(p1[0], p1[1]), u0 = cvtpk(p0[0], p0[1]);
      f32x2 s = f32x2{blo(u3), bhi(u3)} + f32x2{blo(u2), bhi(u2)};
      u32 us = cvtpk(s[0], s[1]);
      s = f32x2{blo(us), bhi(us)} + f32x2{blo(u1), bhi(u1)}; us = cvtpk(s[0], s[1]);
      s = f32x2{blo(us), bhi(us)} + f32x2{blo(u0), bhi(u0)}; us = cvtpk(s[0], s[1]);
      s = f32x2{blo(us), bhi(us)} * f32x2{blo(in.v[e][cp]), bhi(in.v[e][cp])};
      xkv[e][cp] = cvtpk(s[0], s[1]);
    }
  }
#pragma unroll
  for (int arr = 0; arr < 2; arr++) {
#pragma unroll
    for (int a = 0; a < 2; a++) {
      u32x2 x = arr ? xkv[a] : xu[a];
      u32 keep = odd2 ? x[1] : x[0], send = odd2 ? x[0] : x[1];
      u32 other = __builtin_amdgcn_update_dpp(0u, send, 0x4e, 0xf, 0xf, true);
      lds_w32(xw + arr * 4 * XB_STRIDE + a * XP_STRIDE, perm(other, keep, sel0));
      lds_w32(xw + (arr * 4 + 1) * XB_STRIDE + a * XP_STRIDE, perm(other, keep, sel1));
    }
  }
}

DI void epilogue(lptr yr, u32 ooff, u32 tag, const u64* gp, u64 gr, const u32x2 (&xu)[2], const u32x2 (&xkv)[2], u32x2 dsk,
                 u32x2 dss, rsrc_t rout) {
  u32 ya[4], yb[4];
#pragma unroll
  for (int c = 0; c < 4; c++) {
    ya[c] = *(LDS u32*)(yr + c * KB_STRIDE);
    yb[c] = *(LDS u32*)(yr + (4 + c) * KB_STRIDE);
  }
  if (!__all((u32)(gr >> 32) == tag)) {
#pragma unroll 1
    for (int it = 0; it < (1 << 16); it++) {
      __builtin_amdgcn_s_sleep(2);
      gr = __hip_atomic_load(gp, __ATOMIC_RELAXED, __HIP_MEMORY_SCOPE_AGENT);
      if (__all((u32)(gr >> 32) == tag)) break;
    }
  }
#pragma unroll
  for (int e = 0; e < 2; e++) {
    float g = e ? bhi((u32)gr) : blo((u32)gr), g1 = rb(1.f - g);
    u32x2 o;
#pragma unroll
    for (int cp = 0; cp < 2; cp++) {
      float res[2];
#pragma unroll
      for (int hl = 0; hl < 2; hl++) {
        int c = cp * 2 + hl;
        float uu = hl ? bhi(xu[e][cp]) : blo(xu[e][cp]);
        float kv = hl ? bhi(xkv[e][cp]) : blo(xkv[e][cp]);
        float d1 = hl ? bhi(dsk[cp]) : blo(dsk[cp]);
        float d2 = hl ? bhi(dss[cp]) : blo(dss[cp]);
        float y1 = e ? bhi(ya[c]) : blo(ya[c]);
        float y2 = e ? bhi(yb[c]) : blo(yb[c]);
        float a = rb(y1 + rb(uu * d1));
        float bb = rb(uu * rb(y2 + rb(kv * d2)));
        res[hl] = rb(g * a) + rb(g1 * bb);
      }
      o[cp] = cvtpk(res[0], res[1]);
    }
    __builtin_amdgcn_raw_buffer_store_b64(__builtin_bit_cast(__attribute__((ext_vector_type(2))) unsigned, o), rout, ooff + e * 2048, 0, 0);
  }
}

template <class F> DI void conv(const u32x4 (&A)[16], u32 xa, lptr yw, int rot, int lane, lptr cnt, F hook) {
  Acc acc; BF b0; float carry[3];
  const bool l0 = lane == 0;
  cnt_wait(cnt + 0, 4);
  block<0, 0, 1, false, 0>(A, acc, b0, xa);
  finish<0>(acc[0], carry, yw, rot, l0);
  cnt_signal(cnt + 16, lane);
  hook(std::integral_constant<int, 0>{});
  block<0, 2, -1, true, -1>(A, acc, b0, xa);
  hook(std::integral_constant<int, 1>{});
  cnt_wait(cnt + 4, 4);
  block<1, 2, 1, false, 0>(A, acc, b0, xa);
  finish<1>(acc[1], carry, yw, rot, l0);
  cnt_signal(cnt + 20, lane);
  hook(std::integral_constant<int, 2>{});
  block<0, -1, 3, true, 4>(A, acc, b0, xa);
  hook(std::integral_constant<int, 3>{});
  block<1, -1, 3, true, -1>(A, acc, b0, xa);
  hook(std::integral_constant<int, 4>{});
  cnt_wait(cnt + 8, 4);
  block<2, 2, 3, false, -1>(A, acc, b0, xa);
  finish<2>(acc[0], carry, yw, rot, l0);
  cnt_signal(cnt + 24, lane);
  hook(std::integral_constant<int, 5>{});
  cnt_wait(cnt + 12, 4);
  block<3, -1, 3, false, -1>(A, acc, b0, xa);
  finish<3>(acc[1], carry, yw, rot, l0);
  cnt_signal(cnt + 28, lane);
}

__global__ __launch_bounds__(512, 2) void conv_kernel(
    const void* __restrict__ gu, const void* __restrict__ logAr, const void* __restrict__ Aim, const void* __restrict__ Cre,
    const void* __restrict__ Cim, const void* __restrict__ logdt, const void* __restrict__ Dskip, const void* __restrict__ gk,
    const void* __restrict__ gv, const void* __restrict__ shk, const void* __restrict__ ssmk, const void* __restrict__ ssmD,
    const void* __restrict__ gatew, void* ws, void* __restrict__ out) {
  extern __shared__ __attribute__((aligned(16))) char smem_[];
  lptr smem = (lptr)smem_;
  const int b = blockIdx.x, cg = (b & 7) * 32 + (b >> 3);
  const int tid = threadIdx.x, lane = tid & 63, w = __builtin_amdgcn_readfirstlane(tid >> 6);
  const int ch = cg * 4 + (w & 3);
  const int j16 = lane & 15, q = lane >> 4;
  const bool isA = w < 4;
  lptr kb = smem + OFF_K + w * KB_STRIDE;
  u64* gq = (u64*)ws;
  u32* gepoch = (u32*)((char*)ws + 65536);
  const rsrc_t ru = mkrsrc(gu, L * D * 2), rk = mkrsrc(gk, L * D * 2), rv = mkrsrc(gv, L * D * 2), rout = mkrsrc(out, L * D * 2);
  STAMP(0);
  lptr cnt = smem + OFF_MISC;
  u64 pc;
  asm volatile("s_getpc_b64 %0" : "=s"(pc));
  if (tid < 16) lds_w32(cnt + tid * 4, 0);
  asm volatile("s_waitcnt lgkmcnt(0)\n\ts_barrier" ::: "memory");
  auto loadA = [&](u32x4 (&A)[16]) {
    const int m = lane & 3, ep = (lane >> 2) & 3;
    lptr ka = kb + 32 + (-12 + 4 * m + 4 * (8 * q + 7 - ep)) * 2;
#pragma unroll
    for (int c = 0; c < 16; c++) {
      u32x2 lo = lds_tr(ka + c * 256), hi = lds_tr(ka + c * 256 - 32);
      A[c] = u32x4{lo[0], lo[1], hi[0], hi[1]};
    }
  };
  const u32 xa = (u32)(size_t)(smem + OFF_X + w * XB_STRIDE + (64 - 32 + 4 * j16 - 8 * q - 4) * 2);
  const int rot = (lane == 0 ? 63 : q == 0 ? lane + 47 : lane - 16) * 4;
  lptr yw = kb + 32 + (16 * j16 + 4 * q) * 2;
  u32x4 A[16];
  const int tb = tid & 255, wb = w & 3;
  const u32 xoff = (2 * tb * 256 + cg) * 8;
  u32x2 xu[4][2], xkv[4][2];
  XIn in[4];
  auto load_x = [&](int s) {
    const u32 o = xoff + s * 512 * 2048;
    const int hrow = 512 * s + 128 * wb - 3 + lane;
    in[s].k[0] = bl64(rk, o); in[s].k[1] = bl64(rk, o + 2048);
    in[s].h = bl64(rk, (lane < 3 && hrow >= 0) ? (u32)(hrow * 256 + cg) * 8 : OOB);
    in[s].v[0] = bl64(rv, o); in[s].v[1] = bl64(rv, o + 2048);
    xu[s][0] = bl64(ru, o); xu[s][1] = bl64(ru, o + 2048);
    __builtin_amdgcn_sched_barrier(0);
  };
  auto landed = [&](int s) { asm volatile("" : "+v"(xu[s][1])); __builtin_amdgcn_sched_barrier(0); };
  const bool odd2 = lane & 2;
  const u32 sel0 = odd2 ? 0x01000504u : 0x05040100u, sel1 = odd2 ? 0x03020706u : 0x07060302u;
  lptr xw = smem + OFF_X + (odd2 ? 2 * XB_STRIDE : 0) + (lane & 1) * 2 * XP_STRIDE + 128 + (tb >> 2) * 4;
  u32x4 shw0, shw1; u32x2 dsk, dss;
  auto load_par = [&]() {
    const u32* p1 = (const u32*)shk + cg * 8;
#pragma unroll
    for (int e = 0; e < 4; e++) { shw0[e] = p1[e]; shw1[e] = p1[4 + e]; }
#pragma unroll
    for (int e = 0; e < 2; e++) { dsk[e] = ((const u32*)Dskip)[cg * 2 + e]; dss[e] = ((const u32*)ssmD)[cg * 2 + e]; }
  };
  auto proc = [&](int s) {
    process_x(xw + s * 256, sel0, sel1, odd2, xu[s], in[s], shw0, shw1, xkv[s]);
    cnt_signal(cnt + 4 * s, lane);
  };
  lptr yr = smem + OFF_K + 32 + tb * 4;
  const u64* gp = gq + tb;
  u64 gr[4];
  auto ldg = [&](int s) { gr[s] = __hip_atomic_load(gp + s * 256, __ATOMIC_RELAXED, __HIP_MEMORY_SCOPE_AGENT); };
  auto epi = [&](int s, u32 tag) {
    cnt_wait(cnt + 16 + 4 * s, 8);
    epilogue(yr + s * 1024, xoff + s * 512 * 2048, tag, gp + s * 256, gr[s], xu[s], xkv[s], dsk, dss, rout);
  };
  auto retire = [&](u32 tag) {
    if (b == 0 && lane == 0) {
      u32 n = __hip_atomic_fetch_add((LDS u32*)(cnt + 32), 1u, __ATOMIC_RELAXED, __HIP_MEMORY_SCOPE_WORKGROUP);
      if (n == 7) __hip_atomic_store(gepoch, tag, __ATOMIC_RELAXED, __HIP_MEMORY_SCOPE_AGENT);
    }
  };

  if (isA) {
    const rsrc_t r1 = mkrsrc(logAr, D * NM * 2), r2 = mkrsrc(Aim, D * NM * 2), r3 = mkrsrc(Cre, D * NM * 2), r4 = mkrsrc(Cim, D * NM * 2);
    const u32 po = (ch * 64 + lane) * 4;
    u32 pAr = bl32(r1, po), pAi = bl32(r2, po), pCr = bl32(r3, po), pCi = bl32(r4, po);
    const u32 pdt = (((const u32*)logdt)[ch >> 1] >> ((ch & 1) * 16)) & 0xffffu;
    load_par();
    const rsrc_t rw = mkrsrc(gatew, D * 2);
    const int grow = b * 8 + wb * 2;
    u32x4 gx[4], gw[2];
#pragma unroll
    for (int e = 0; e < 4; e++) gx[e] = bl128(ru, grow * 2048 + (e * 64 + lane) * 16);
#pragma unroll
    for (int e = 0; e < 2; e++) gw[e] = bl128(rw, (e * 64 + lane) * 16);
    const u32 tag = __hip_atomic_load(gepoch, __ATOMIC_RELAXED, __HIP_MEMORY_SCOPE_AGENT) + 1;
    __builtin_amdgcn_sched_barrier(0);
    auto gate = [&]() {
      u32 gb[2];
#pragma unroll
      for (int rr = 0; rr < 2; rr++) {
        float s = 0.f;
#pragma unroll
        for (int h = 0; h < 2; h++) {
          u32x4 x = gx[rr * 2 + h], ww = gw[h];
#pragma unroll
          for (int e = 0; e < 4; e++) { u32 p = cvtpk(blo(x[e]) * blo(ww[e]), bhi(x[e]) * bhi(ww[e])); s += blo(p) + bhi(p); }
        }
        s = wave_sum(s);
        float z = rb(s), g = 1.f / (1.f + expf(-z));
        gb[rr] = cvtpk(g, g) & 0xffffu;
      }
      if (lane == 0) __hip_atomic_store(gq + (grow >> 1), ((u64)tag << 32) | (gb[0] | gb[1] << 16), __ATOMIC_RELAXED, __HIP_MEMORY_SCOPE_AGENT);
    };
    if (w == 0) {
      __builtin_amdgcn_raw_ptr_buffer_load_lds(mkrsrc((const void*)(pc & ~127ull), CODE_BYTES), (LDS void*)(smem + OFF_PF + 4096), 4,
                                               lane < 10 ? ((b >> 3) * 10 + lane) * 128 : OOB, 0, 0, 0);
    }
    STAMP(10);
    if (PRIO & 1) __builtin_amdgcn_s_setprio(1);
    float dt = rb(expf(bf(pdt)));
    f32x2 zr, zi; cplx C;
    zr[0] = -rb(expf(blo(pAr))) * dt; zr[1] = -rb(expf(bhi(pAr))) * dt;
    zi[0] = blo(pAi) * dt; zi[1] = bhi(pAi) * dt;
    C.r[0] = 2.f * blo(pCr) * dt; C.r[1] = 2.f * bhi(pCr) * dt;
    C.i[0] = -2.f * blo(pCi) * dt; C.i[1] = -2.f * bhi(pCi) * dt;
    zr *= 1.44269504088896341f; zi *= 0.15915494309189535f;
    STAMP(11);
    lptr tb = smem + OFF_T + w * TB_STRIDE;
    lptr tw = tb + lane * 8, tr = tb + j16 * TB_ROW + q * 16;
    cplx w1 = cexp_p(zr, zi, 1.f), w16 = cexp_p(zr, zi, 16.f), w256 = cexp_p(zr, zi, 256.f);
    u32x4 G[8];
    {
      cplx g; g.r = f32x2{1.f, 1.f}; g.i = f32x2{0.f, 0.f};
      tput(tw, g); g = w16; tput(tw + TB_ROW, g);
#pragma unroll
      for (int i = 2; i < 16; i++) { g = cmul(g, w16); tput(tw + i * TB_ROW, g); }
      wsync();
#pragma unroll
      for (int s = 0; s < 8; s++) G[s] = lds_r128(tr + s * 64);
      wsync();
    }
    STAMP(12);
    w1 = conj(w1); w256 = conj(w256);
    cplx w2 = csq(w1), w4 = csq(w2), w8 = csq(w4), w512 = csq(w256), w1024 = csq(w512);
    lptr kd = kb + 32;
    if (lane < 8) lds_w32(kb + lane * 4, 0);
    {
      cplx e = C; tput(tw, e);
#pragma unroll
      for (int i = 1; i < 16; i++) { e = cmul(e, w1); tput(tw + i * TB_ROW, e); }
      f32x4 a = {0, 0, 0, 0};
      wsync();
#pragma unroll
      for (int s = 0; s < 8; s++) a = mfma(lds_r128(tr + s * 64), G[s], a);
      wsync();
      lds_w64(kd + (16 * j16 + 4 * q) * 2, u32x2{cvtpk(a[0], a[1]), cvtpk(a[2], a[3])});
    }
    STAMP(13);
    {
      cplx e = cmul(C, w256); tput(tw, e);
#pragma unroll
      for (int i = 1; i < 8; i++) { e = cmul(e, w2); tput(tw + i * TB_ROW, e); }
      e = cmul(C, w512);
#pragma unroll
      for (int i = 0; i < 4; i++) { tput(tw + (8 + i) * TB_ROW, e); tput(tw + (12 + i) * TB_ROW, cmul(e, w256)); if (i < 3) e = cmul(e, w4); }
      f32x4 a = {0, 0, 0, 0};
      wsync();
#pragma unroll
      for (int s = 0; s < 8; s++) a = mfma(lds_r128(tr + s * 64), G[s], a);
      wsync();
      u32 p01 = cvtpk(a[0], a[1]), p23 = cvtpk(a[2], a[3]);
      u32 v00 = perm(p01, p01, 0x01000100), v11 = perm(p01, p01, 0x03020302), v22 = perm(p23, p23, 0x01000100), v33 = perm(p23, p23, 0x03020302);
      const int base = q < 2 ? 256 + 16 * j16 + 8 * q : 512 + 256 * (q - 2) + 16 * j16;
      lds_w128(kd + base * 2, q < 2 ? u32x4{v00, perm(p23, p01, 0x05040302), v22, v33} : u32x4{v00, p01, v11, v22});
      if (q >= 2) lds_w128(kd + base * 2 + 16, u32x4{v22, p23, v33, v33});
      wsync();
      if (q >= 2 && base > 512) lds_w32(kd + (base - 2) * 2, v00); else if (base > 256) lds_w16(kd + (base - 1) * 2, (u16)p01);
      wsync();
    }
    STAMP(14);
    {
      cplx e0 = cmul(C, w1024), e1 = cmul(e0, w8);
#pragma unroll
      for (int i = 0; i < 4; i++) { tput(tw + (2 * i) * TB_ROW, e0); tput(tw + (2 * i + 1) * TB_ROW, e1); e0 = cmul(e0, w256); if (i < 3) e1 = cmul(e1, w256); }
      tput(tw + 8 * TB_ROW, e0);
      f32x4 a = {0, 0, 0, 0};
      wsync();
#pragma unroll
      for (int s = 0; s < 8; s++) a = mfma(lds_r128(tr + s * 64), G[s], a);
      wsync();
      u32 p01 = cvtpk(a[0], a[1]), p23 = cvtpk(a[2], a[3]);
      if (q < 2) {
#pragma unroll
        for (int hh = 0; hh < 2; hh++) {
          u32 p = hh ? p23 : p01;
          u32 v00 = perm(p, p, 0x01000100), v11 = perm(p, p, 0x03020302);
          int base = 1024 + 512 * q + 256 * hh + 16 * j16;
          lds_w128(kd + base * 2, u32x4{v00, v00, p, v11});
          lds_w128(kd + base * 2 + 16, u32x4{v11, v11, v11, v11});
        }
      }
      wsync();
      if (q < 2) {
#pragma unroll
        for (int hh = 0; hh < 2; hh++) {
          u32 p = hh ? p23 : p01;
          u32 v00 = perm(p, p, 0x01000100);
          int base = 1024 + 512 * q + 256 * hh + 16 * j16;
          if (base > 1024) lds_w64(kd + (base - 4) * 2, u32x2{v00, v00}); else lds_w32(kd + (base - 2) * 2, v00);
        }
      } else if (lane == 32) {
        u32 v00 = perm(p01, p01, 0x01000100);
        lds_w64(kd + 2044 * 2, u32x2{v00, v00});
      }
      wsync();
    }
    STAMP(15);
    if (PRIO & 1) __builtin_amdgcn_s_setprio(0);
    if (lane == 0) lds_w32(cnt + 36, tag);
    loadA(A);
    STAMP(1);
    conv(A, xa, yw, rot, lane, cnt, [&](auto k) {
      constexpr int K = k.value;
      STAMP(2 + K);
      if (PRIO & 8) __builtin_amdgcn_s_setprio(1);
      if (K == GATE_AT) gate();
      if (K == X3_AT) load_x(3);
      if (K == P3_AT) proc(3);
      if (K == 4) ldg(3);
      if (PRIO & 8) __builtin_amdgcn_s_setprio(0);
    });
    STAMP(8);
    epi(3, tag);
    retire(tag);
    STAMP(9);
    STAMP_DUMP();
  } else {
    const rsrc_t rs = mkrsrc(ssmk, L * D * 2);
    load_x(0);
    u32x4 kk[4];
#pragma unroll
    for (int e = 0; e < 4; e++) kk[e] = bl128(rs, ch * 4096 + (e * 64 + lane) * 16);
    load_par();
    __builtin_amdgcn_sched_barrier(0);
    STAMP(10);
    for (int i = tb; i < 32 * 32; i += 256) lds_w32(smem + OFF_X + (i >> 7) * XB_STRIDE + ((i >> 5) & 3) * XP_STRIDE + (i & 31) * 4, 0);
    if (lane < 8) lds_w32(kb + lane * 4, 0);
    STAMP(11);
    landed(0);
    STAMP(12);
    load_x(1);
    proc(0);
    STAMP(13);
#pragma unroll
    for (int e = 0; e < 4; e++) lds_w128(kb + 32 + (e * 64 + lane) * 16, kk[e]);
    wsync();
    STAMP(14);
    STAMP(15);
    loadA(A);
    STAMP(1);
    u32 tag = 0;
    if (PRIO & 2) __builtin_amdgcn_s_setprio(1);
    conv(A, xa, yw, rot, lane, cnt, [&](auto k) {
      constexpr int K = k.value;
      STAMP(2 + K);
      if (PRIO & 4) __builtin_amdgcn_s_setprio(1);
      if (K == 0) { landed(1); load_x(2); proc(1); }
      if (K == 2) proc(2);
      if (K == 3) { ldg(0); ldg(1); ldg(2); }
      if ((PRIO & 16) && K == PDROP) __builtin_amdgcn_s_setprio(0);
      if (K == 4) {
        cnt_wait(cnt + 16, 8);
        tag = __builtin_amdgcn_readfirstlane(*(LDS u32*)(cnt + 36));
        epi(0, tag);
      }
      if (K == 5) epi(1, tag);
      if (PRIO & 4) __builtin_amdgcn_s_setprio(0);
    });
    STAMP(8);
    epi(2, tag);
    retire(tag);
    STAMP(9);
    STAMP_DUMP();
  }
}

extern "C" void kernel_launch(void* const* d_in, const int* in_sizes, int n_in, void* d_out, int out_size, void* d_ws,
                              size_t ws_size, hipStream_t stream) {
  (void)hipFuncSetAttribute((const void*)conv_kernel, hipFuncAttributeMaxDynamicSharedMemorySize, LDS_BYTES);
  conv_kernel<<<256, 512, LDS_BYTES, stream>>>(d_in[0], d_in[1], d_in[2], d_in[3], d_in[4], d_in[5], d_in[6], d_in[8], d_in[9],
                                               d_in[10], d_in[11], d_in[12], d_in[13], d_ws, d_out);
}
```
